```python
import torch

def _softmax(x, axis=-1):
    return torch.softmax(x, dim=axis)
def _gelu(x):
    return torch.nn.functional.gelu(x, approximate="tanh")
def _silu(x):
    return torch.nn.functional.silu(x)
def _sigmoid(x):
    return torch.sigmoid(x)
def _layernorm(x, axis=-1, eps=1e-5):
    m = x.mean(dim=axis, keepdim=True)
    v = x.var(dim=axis, keepdim=True, unbiased=False)
    return (x - m) / torch.sqrt(v + eps)
def _scatter_add(tgt, idx, src, axis=0):
    return tgt.clone().index_add_(axis, idx, src)
def _segment_sum(src, seg, n):
    out = torch.zeros((n,) + src.shape[1:], dtype=src.dtype)
    return out.index_add_(0, seg, src)
def _pad(x, pw):
    flat = tuple(v for pair in reversed(pw) for v in pair)
    return torch.nn.functional.pad(x, flat)

kernel_name = 'standalone__sliding_block__prompt'

def setup_inputs():

    _w = (lambda a: (lambda t: t.to(torch.bfloat16) if t.is_floating_point() else t)(torch.as_tensor(a)))
    B, S, D = 1, 2048, 1024
    return {
        "x": _w((lambda *s: torch.randn(*s, dtype=torch.bfloat16) * 0.2)(B, S, D)),
        "wqkv": _w((lambda *s: torch.randn(*s, dtype=torch.bfloat16) * 0.2)(D, 3 * D)),
        "wo": _w((lambda *s: torch.randn(*s, dtype=torch.bfloat16) * 0.05)(D, D)),
    }

def reference(x, wqkv, wo):
    B, S, D = x.shape
    H = 32; Dh = D // H; W = 256
    qkv = (lambda x, *d: x.permute(*d))((x @ wqkv).reshape(B, S, 3, H, Dh), 2, 0, 3, 1, 4)
    q, k, v = qkv[0], qkv[1], qkv[2]
    scores = torch.einsum("bhqd,bhkd->bhqk", q, k) * (Dh ** -0.5)
    idx = torch.arange(S)
    mask = (idx[:, None] - idx[None, :] >= 0) & (idx[:, None] - idx[None, :] < W)
    scores = torch.where(mask[None, None], scores, -1e9)
    p = _softmax(scores, axis=-1)
    o = torch.einsum("bhqk,bhkd->bhqd", p, v)
    return (lambda x, *d: x.permute(*d))(o, 0, 2, 1, 3).reshape(B, S, D) @ wo
```

```cpp
#include <hip/hip_runtime.h>
#include <hip/hip_bf16.h>
#include <stdint.h>

typedef __bf16 bf16x8 __attribute__((ext_vector_type(8)));
typedef __bf16 bf16x2 __attribute__((ext_vector_type(2)));
typedef short s16x4 __attribute__((ext_vector_type(4)));
typedef float f32x16 __attribute__((ext_vector_type(16)));
typedef float f32x4 __attribute__((ext_vector_type(4)));
typedef float f32x2 __attribute__((ext_vector_type(2)));
typedef unsigned u32x4 __attribute__((ext_vector_type(4)));
typedef unsigned u32x2 __attribute__((ext_vector_type(2)));
typedef __attribute__((address_space(3))) char lds_char;

#define DI __device__ __forceinline__
#define MFMA32(a, b, c) __builtin_amdgcn_mfma_f32_32x32x16_bf16((a), (b), (c), 0, 0, 0)
#define WAIT_V(n) asm volatile("s_waitcnt vmcnt(" #n ")" ::: "memory")
#define BAR() __builtin_amdgcn_s_barrier()

constexpr int S_LEN = 2048, DM = 1024, NH = 32;

DI unsigned pk2(float a, float b) {
  f32x2 v = {a, b};
  bf16x2 r = __builtin_convertvector(v, bf16x2);
  return __builtin_bit_cast(unsigned, r);
}
DI bf16x8 pack8(const f32x16& a, int s) {
  u32x4 r;
  r[0] = pk2(a[8 * s + 0], a[8 * s + 1]);
  r[1] = pk2(a[8 * s + 2], a[8 * s + 3]);
  r[2] = pk2(a[8 * s + 4], a[8 * s + 5]);
  r[3] = pk2(a[8 * s + 6], a[8 * s + 7]);
  return __builtin_bit_cast(bf16x8, r);
}
DI bf16x8 lds_b128(lds_char* p) { return *(__attribute__((address_space(3))) bf16x8*)p; }
DI bf16x8 lds_tr2(lds_char* p0, lds_char* p1) {
  s16x4 a = __builtin_amdgcn_ds_read_tr16_b64_v4i16((__attribute__((address_space(3))) s16x4*)p0);
  s16x4 b = __builtin_amdgcn_ds_read_tr16_b64_v4i16((__attribute__((address_space(3))) s16x4*)p1);
  u32x2 ua = __builtin_bit_cast(u32x2, a), ub = __builtin_bit_cast(u32x2, b);
  u32x4 r = {ua[0], ua[1], ub[0], ub[1]};
  return __builtin_bit_cast(bf16x8, r);
}
DI void glds16(__amdgpu_buffer_rsrc_t rs, lds_char* dst, unsigned voff, unsigned soff) {
  unsigned m = (unsigned)(size_t)dst;
  asm volatile("s_mov_b32 m0, %0\n\ts_nop 0\n\tbuffer_load_dwordx4 %1, %2, %3 offen lds"
               :: "s"(m), "v"(voff), "s"(rs), "s"(soff) : "m0", "memory");
}

constexpr int G1_STAGE = 40960, G1_NS = 4, G1_XB = 16384;
__global__ __launch_bounds__(512, 2) void k_qkv(const __hip_bfloat16* __restrict__ x,
                                                const __hip_bfloat16* __restrict__ w,
                                                char* __restrict__ qkv) {
  extern __shared__ __attribute__((aligned(16))) char smem_[];
  lds_char* smem = (lds_char*)smem_;
  const int tid = threadIdx.x, lane = tid & 63;
  const int wv = __builtin_amdgcn_readfirstlane(tid >> 6);
  const int b = blockIdx.x, xcd = b & 7, idx = b >> 3;
  const int rb = (xcd >> 2) * 8 + (idx & 7);
  const int hp = (xcd & 3) * 4 + (idx >> 3);
  __amdgpu_buffer_rsrc_t rx = __builtin_amdgcn_make_buffer_rsrc((void*)x, 0, S_LEN * DM * 2, 0x00020000);
  __amdgpu_buffer_rsrc_t rw = __builtin_amdgcn_make_buffer_rsrc((void*)w, 0, DM * 3 * DM * 2, 0x00020000);
  unsigned vx[2], vw[3];
#pragma unroll
  for (int i = 0; i < 2; ++i) {
    int row = (2 * wv + i) * 8 + (lane >> 3);
    int lc = (lane & 7) ^ ((row >> 1) & 7);
    vx[i] = (unsigned)((rb * 128 + row) * 2048 + lc * 16);
  }
#pragma unroll
  for (int i = 0; i < 3; ++i) {
    int pi = 3 * wv + i, seg = pi >> 3, k = (pi & 7) * 8 + (lane >> 3);
    int lc = (lane & 7) ^ (((k >> 1) & 1) << 2);
    vw[i] = (unsigned)(k * 6144 + seg * 2048 + hp * 128 + lc * 16);
  }
  auto stage = [&](int kt, int slot) {
    lds_char* base = smem + slot * G1_STAGE;
#pragma unroll
    for (int i = 0; i < 2; ++i) glds16(rx, base + (2 * wv + i) * 1024, vx[i], kt * 128);
#pragma unroll
    for (int i = 0; i < 3; ++i) glds16(rw, base + G1_XB + (3 * wv + i) * 1024, vw[i], kt * 64 * 6144);
  };
  const int qb = wv & 3, hh = wv >> 2;
  const int r = lane & 31, h = lane >> 5;
  const int arow = qb * 32 + r;
  const unsigned aoff = arow * 128 + ((h ^ ((arow >> 1) & 7)) << 4);
  const int q4 = (lane & 15) >> 2, p4 = lane & 3, cb = (lane >> 4) & 1;
  const unsigned boff = G1_XB + (8 * h + q4) * 128 + (((hh * 4 + cb * 2 + (p4 >> 1)) ^ ((q4 >> 1) << 2)) << 4) + 8 * (p4 & 1);

  f32x16 aq = {}, ak = {}, av = {};
  stage(0, 0); stage(1, 1); stage(2, 2);
  for (int kt = 0; kt < 16; ++kt) {
    if (kt < 14) WAIT_V(10); else if (kt == 14) WAIT_V(5); else WAIT_V(0);
    BAR();
    if (kt + 3 < 16) stage(kt + 3, (kt + 3) & 3);
    lds_char* base = smem + (kt & 3) * G1_STAGE;
#pragma unroll
    for (int s = 0; s < 4; ++s) {
      bf16x8 a = lds_b128(base + (aoff ^ (s << 5)));
      lds_char* bp = base + boff + s * 2048;
      bf16x8 wq = lds_tr2(bp, bp + 512);
      bf16x8 wk = lds_tr2(bp + 8192, bp + 8192 + 512);
      bf16x8 wvv = lds_tr2(bp + 16384, bp + 16384 + 512);
      aq = MFMA32(wq, a, aq);
      ak = MFMA32(wk, a, ak);
      av = MFMA32(a, wvv, av);
    }
  }
  const float qs = 0.17677669529663687f * 1.4426950408889634f;
#pragma unroll
  for (int i = 0; i < 16; ++i) aq[i] *= qs;
  const int head = hp * 2 + hh, g = rb * 4 + qb;
  char* dst = qkv + ((size_t)(head * 64 + g) * 6) * 1024 + lane * 16;
  *(bf16x8*)(dst + 0 * 1024) = pack8(aq, 0);
  *(bf16x8*)(dst + 1 * 1024) = pack8(aq, 1);
  *(bf16x8*)(dst + 2 * 1024) = pack8(ak, 0);
  *(bf16x8*)(dst + 3 * 1024) = pack8(ak, 1);
  *(bf16x8*)(dst + 4 * 1024) = pack8(av, 0);
  *(bf16x8*)(dst + 5 * 1024) = pack8(av, 1);
}

DI float xhalf_max(float v) {
  auto rr = __builtin_amdgcn_permlane32_swap(__float_as_uint(v), __float_as_uint(v), false, false);
  return fmaxf(__uint_as_float(rr[0]), __uint_as_float(rr[1]));
}
DI float xhalf_sum(float v) {
  auto rr = __builtin_amdgcn_permlane32_swap(__float_as_uint(v), __float_as_uint(v), false, false);
  return __uint_as_float(rr[0]) + __uint_as_float(rr[1]);
}

__global__ __launch_bounds__(512, 2) void k_attn(const char* __restrict__ qkv, char* __restrict__ of) {
  extern __shared__ __attribute__((aligned(16))) char smem_[];
  lds_char* smem = (lds_char*)smem_;
  const int tid = threadIdx.x, lane = tid & 63;
  const int wv = __builtin_amdgcn_readfirstlane(tid >> 6);
  const int b = blockIdx.x;
  const int head = b & 31, rb = b >> 5;
  const int qb = wv < 4 ? wv : 11 - wv;
  const int g = rb * 8 + qb;
  __amdgpu_buffer_rsrc_t rq = __builtin_amdgcn_make_buffer_rsrc((void*)qkv, 0, NH * 64 * 6144, 0x00020000);
#pragma unroll
  for (int i = 0; i < 2; ++i) {
    int j = wv * 2 + i, kg = rb * 8 - 8 + j;
    if (kg >= 0) {
#pragma unroll
      for (int c = 0; c < 4; ++c)
        glds16(rq, smem + j * 4096 + c * 1024, lane * 16, (head * 64 + kg) * 6144 + 2048 + c * 1024);
    }
  }
  const char* qp = qkv + (size_t)(head * 64 + g) * 6144 + lane * 16;
  bf16x8 qf0 = *(const bf16x8*)(qp), qf1 = *(const bf16x8*)(qp + 1024);
  WAIT_V(0);
  BAR();
  const int r = lane & 31, h = lane >> 5;
  lds_char* kb = smem + qb * 4096 + lane * 16;
  f32x16 sc[9];
  const float NEG = -INFINITY;
#pragma unroll
  for (int j = 0; j < 9; ++j) {
    if (g - 8 + j >= 0) {
      f32x16 z = {};
      bf16x8 k0 = lds_b128(kb + j * 4096), k1 = lds_b128(kb + j * 4096 + 1024);
      z = MFMA32(k0, qf0, z);
      z = MFMA32(k1, qf1, z);
      sc[j] = z;
    } else {
#pragma unroll
      for (int i = 0; i < 16; ++i) sc[j][i] = NEG;
    }
  }
#pragma unroll
  for (int i = 0; i < 16; ++i) {
    int key = (i & 3) + 8 * (i >> 2) + 4 * h;
    if (!(key > r)) sc[0][i] = NEG;
    if (key > r) sc[8][i] = NEG;
  }
  float m = sc[8][0];
#pragma unroll
  for (int j = 0; j < 9; ++j)
#pragma unroll
    for (int i = 0; i < 16; ++i) m = fmaxf(m, sc[j][i]);
  m = xhalf_max(m);
  float l = 0.f;
  f32x16 o = {};
#pragma unroll
  for (int j = 0; j < 9; ++j) {
    if (g - 8 + j >= 0) {
#pragma unroll
      for (int i = 0; i < 16; ++i) {
        float p = __builtin_amdgcn_exp2f(sc[j][i] - m);
        sc[j][i] = p;
        l += p;
      }
      bf16x8 v0 = lds_b128(kb + j * 4096 + 2048), v1 = lds_b128(kb + j * 4096 + 3072);
      o = MFMA32(v0, pack8(sc[j], 0), o);
      o = MFMA32(v1, pack8(sc[j], 1), o);
    }
  }
  l = xhalf_sum(l);
  const float inv = 1.0f / l;
#pragma unroll
  for (int i = 0; i < 16; ++i) o[i] *= inv;
  char* dst = of + (size_t)(g * 32 + head) * 2048 + lane * 16;
  *(bf16x8*)(dst) = pack8(o, 0);
  *(bf16x8*)(dst + 1024) = pack8(o, 1);
}

__global__ __launch_bounds__(512, 2) void k_oproj(const char* __restrict__ of, const __hip_bfloat16* __restrict__ wo,
                                                  __hip_bfloat16* __restrict__ out) {
  extern __shared__ __attribute__((aligned(16))) char smem_[];
  lds_char* smem = (lds_char*)smem_;
  const int tid = threadIdx.x, lane = tid & 63;
  const int wv = __builtin_amdgcn_readfirstlane(tid >> 6);
  const int b = blockIdx.x, xcd = b & 7, idx = b >> 3;
  const int rb = xcd * 2 + (idx & 1), cbk = idx >> 1;
  __amdgpu_buffer_rsrc_t rw = __builtin_amdgcn_make_buffer_rsrc((void*)wo, 0, DM * DM * 2, 0x00020000);
  {
    const int k = lane >> 3;
    const unsigned vo = k * 2048 + cbk * 128 + (((lane & 7) ^ (((k >> 1) & 1) << 2)) << 4);
#pragma unroll
    for (int i = 0; i < 16; ++i) {
      int pi = i * 8 + wv;
      glds16(rw, smem + pi * 1024, vo, pi * 8 * 2048);
    }
  }
  const int qb = wv & 3, kh = wv >> 2;
  const char* op = of + (size_t)((rb * 4 + qb) * 32 + kh * 16) * 2048 + lane * 16;
  bf16x8 ofr[32];
#pragma unroll
  for (int i = 0; i < 32; ++i) ofr[i] = *(const bf16x8*)(op + i * 1024);
  WAIT_V(0);
  BAR();
  const int h = lane >> 5, q4 = (lane & 15) >> 2, p4 = lane & 3, cb = (lane >> 4) & 1;
  lds_char* bp0 = smem + (kh * 512 + 4 * h + q4) * 128 + ((((cb * 2 + (p4 >> 1))) ^ ((q4 >> 1) << 2)) << 4) + 8 * (p4 & 1);
  lds_char* bp1 = smem + (kh * 512 + 4 * h + q4) * 128 + ((((4 + cb * 2 + (p4 >> 1))) ^ ((q4 >> 1) << 2)) << 4) + 8 * (p4 & 1);
  f32x16 c0 = {}, c1 = {};
#pragma unroll
  for (int i = 0; i < 32; ++i) {
    bf16x8 w0 = lds_tr2(bp0 + i * 2048, bp0 + i * 2048 + 1024);
    bf16x8 w1 = lds_tr2(bp1 + i * 2048, bp1 + i * 2048 + 1024);
    c0 = MFMA32(w0, ofr[i], c0);
    c1 = MFMA32(w1, ofr[i], c1);
  }
  __syncthreads();
  {
    f32x16 snd = kh ? c0 : c1;
    lds_char* xp = smem + wv * 4096 + lane * 16;
#pragma unroll
    for (int i = 0; i < 4; ++i) {
      f32x4 v = {snd[4 * i], snd[4 * i + 1], snd[4 * i + 2], snd[4 * i + 3]};
      *(__attribute__((address_space(3))) f32x4*)(xp + i * 1024) = v;
    }
  }
  __syncthreads();
  f32x16 c = kh ? c1 : c0;
  {
    lds_char* xp = smem + (wv ^ 4) * 4096 + lane * 16;
#pragma unroll
    for (int i = 0; i < 4; ++i) {
      f32x4 v = *(__attribute__((address_space(3))) f32x4*)(xp + i * 1024);
      c[4 * i] += v[0]; c[4 * i + 1] += v[1]; c[4 * i + 2] += v[2]; c[4 * i + 3] += v[3];
    }
  }
  const int r = lane & 31;
  char* dst = (char*)out + (size_t)(rb * 128 + qb * 32 + r) * 2048 + cbk * 128 + kh * 64 + h * 16;
#pragma unroll
  for (int i = 0; i < 2; ++i) {
    unsigned a0 = pk2(c[8 * i + 0], c[8 * i + 1]), a1 = pk2(c[8 * i + 2], c[8 * i + 3]);
    unsigned b0 = pk2(c[8 * i + 4], c[8 * i + 5]), b1 = pk2(c[8 * i + 6], c[8 * i + 7]);
    auto s0 = __builtin_amdgcn_permlane32_swap(a0, b0, false, false);
    auto s1 = __builtin_amdgcn_permlane32_swap(a1, b1, false, false);
    u32x4 v = {s0[0], s1[0], s0[1], s1[1]};
    *(u32x4*)(dst + i * 32) = v;
  }
}

extern "C" void kernel_launch(void* const* d_in, const int* in_sizes, int n_in, void* d_out, int out_size,
                              void* d_ws, size_t ws_size, hipStream_t stream) {
  const __hip_bfloat16* x = (const __hip_bfloat16*)d_in[0];
  const __hip_bfloat16* wqkv = (const __hip_bfloat16*)d_in[1];
  const __hip_bfloat16* wo = (const __hip_bfloat16*)d_in[2];
  char* qkv = (char*)d_ws;
  char* of = qkv + (size_t)NH * 64 * 6144;
  (void)hipFuncSetAttribute((const void*)k_qkv, hipFuncAttributeMaxDynamicSharedMemorySize, G1_STAGE * G1_NS);
  (void)hipFuncSetAttribute((const void*)k_attn, hipFuncAttributeMaxDynamicSharedMemorySize, 65536);
  (void)hipFuncSetAttribute((const void*)k_oproj, hipFuncAttributeMaxDynamicSharedMemorySize, 131072);
  k_qkv<<<256, 512, G1_STAGE * G1_NS, stream>>>(x, wqkv, qkv);
  k_attn<<<256, 512, 65536, stream>>>(qkv, of);
  k_oproj<<<256, 512, 131072, stream>>>(of, wo, (__hip_bfloat16*)d_out);
}
```

```cpp
#include <hip/hip_runtime.h>
#include <hip/hip_bf16.h>
#include <stdint.h>

typedef __bf16 bf16x8 __attribute__((ext_vector_type(8)));
typedef __bf16 bf16x2 __attribute__((ext_vector_type(2)));
typedef short s16x4 __attribute__((ext_vector_type(4)));
typedef float f32x16 __attribute__((ext_vector_type(16)));
typedef float f32x4 __attribute__((ext_vector_type(4)));
typedef float f32x2 __attribute__((ext_vector_type(2)));
typedef unsigned u32x4 __attribute__((ext_vector_type(4)));
typedef unsigned u32x2 __attribute__((ext_vector_type(2)));
typedef __attribute__((address_space(3))) char lds_char;
typedef unsigned long long u64;
typedef __attribute__((address_space(1))) u64 gu64;

#define DI __device__ __forceinline__
#define MFMA32(a, b, c) __builtin_amdgcn_mfma_f32_32x32x16_bf16((a), (b), (c), 0, 0, 0)
#define MFMA16(a, b, c) __builtin_amdgcn_mfma_f32_16x16x32_bf16((a), (b), (c), 0, 0, 0)
#define WAIT_V(n) asm volatile("s_waitcnt vmcnt(" #n ")" ::: "memory")
#define WAIT_L0() __builtin_amdgcn_s_waitcnt(0xC07F)
#define BAR() __builtin_amdgcn_s_barrier()
#define SB0() __builtin_amdgcn_sched_barrier(0)
#define SGB(m, n) __builtin_amdgcn_sched_group_barrier((m), (n), 0)
#define RLX_AGENT __ATOMIC_RELAXED, __HIP_MEMORY_SCOPE_AGENT

constexpr int S_LEN = 2048, DM = 1024;

constexpr size_t WS_EPK = 0;
constexpr size_t WS_EPO = 2048;
constexpr size_t WS_FLK = 4096;
constexpr size_t WS_FLO = 65536;
constexpr size_t WS_KV = 1u << 20;
constexpr size_t WS_OF = WS_KV + (8u << 20);
constexpr size_t WS_ST = WS_OF + (4u << 20);

constexpr int STAGE = 40960, XB = 16384;
constexpr int L_KVL = 65536;
constexpr int L_Q = 98304;
constexpr int L_WP = 114688;
constexpr int L_WL = 0;
constexpr int L_XC = 81920;
constexpr int LDS_BYTES = 163840;
constexpr unsigned SPIN_CAP = 1u << 16;

DI unsigned pk2(float a, float b) {
  f32x2 v = {a, b};
  bf16x2 r = __builtin_convertvector(v, bf16x2);
  return __builtin_bit_cast(unsigned, r);
}
DI bf16x8 pack8(const f32x16& a, int s) {
  u32x4 r;
  r[0] = pk2(a[8 * s + 0], a[8 * s + 1]);
  r[1] = pk2(a[8 * s + 2], a[8 * s + 3]);
  r[2] = pk2(a[8 * s + 4], a[8 * s + 5]);
  r[3] = pk2(a[8 * s + 6], a[8 * s + 7]);
  return __builtin_bit_cast(bf16x8, r);
}
DI bf16x8 lds_b128(lds_char* p) { return *(__attribute__((address_space(3))) bf16x8*)p; }
DI void lds_st128(lds_char* p, u32x4 v) { *(__attribute__((address_space(3))) u32x4*)p = v; }
DI bf16x8 lds_tr2(lds_char* p0, lds_char* p1) {
  s16x4 a = __builtin_amdgcn_ds_read_tr16_b64_v4i16((__attribute__((address_space(3))) s16x4*)p0);
  s16x4 b = __builtin_amdgcn_ds_read_tr16_b64_v4i16((__attribute__((address_space(3))) s16x4*)p1);
  u32x2 ua = __builtin_bit_cast(u32x2, a), ub = __builtin_bit_cast(u32x2, b);
  u32x4 r = {ua[0], ua[1], ub[0], ub[1]};
  return __builtin_bit_cast(bf16x8, r);
}
DI void glds16(__amdgpu_buffer_rsrc_t rs, lds_char* dst, unsigned voff, unsigned soff) {
  unsigned m = (unsigned)(size_t)dst;
  asm volatile("s_mov_b32 m0, %0\n\ts_nop 0\n\tbuffer_load_dwordx4 %1, %2, %3 offen lds"
               :: "s"(m), "v"(voff), "s"(rs), "s"(soff) : "memory");
}
DI float xhalf_max(float v) {
  auto rr = __builtin_amdgcn_permlane32_swap(__float_as_uint(v), __float_as_uint(v), false, false);
  return fmaxf(__uint_as_float(rr[0]), __uint_as_float(rr[1]));
}
DI float xhalf_sum(float v) {
  auto rr = __builtin_amdgcn_permlane32_swap(__float_as_uint(v), __float_as_uint(v), false, false);
  return __uint_as_float(rr[0]) + __uint_as_float(rr[1]);
}
DI float max16(float m, const f32x16& a) {
#pragma unroll
  for (int i = 0; i < 16; i += 2) m = fmaxf(fmaxf(m, a[i]), a[i + 1]);
  return m;
}
DI void wait_words(gu64* p, u64 want) {
#pragma unroll 1
  for (unsigned spins = 0; spins < SPIN_CAP; ++spins) {
    u64 v = __hip_atomic_load(p, RLX_AGENT);
    if (__all(v == want)) break;
    __builtin_amdgcn_s_sleep(1);
  }
  __builtin_amdgcn_fence(__ATOMIC_ACQUIRE, "wavefront");
}
DI u64 uni64(u64 v) {
  unsigned lo = __builtin_amdgcn_readfirstlane((unsigned)v), hi = __builtin_amdgcn_readfirstlane((unsigned)(v >> 32));
  return ((u64)hi << 32) | lo;
}

#define STAMP(i)

struct AttnState { f32x16 o; float m, l; };

DI f32x16 qk_block(lds_char* kp, bf16x8 q0, bf16x8 q1) {
  f32x16 z = {};
  bf16x8 k0 = lds_b128(kp), k1 = lds_b128(kp + 1024);
  z = MFMA32(k0, q0, z);
  z = MFMA32(k1, q1, z);
  return z;
}
DI void pv_block(lds_char* kp, f32x16& sc, float m, float& l, f32x16& o) {
#pragma unroll
  for (int i = 0; i < 16; ++i) {
    float p = __builtin_amdgcn_exp2f(sc[i] - m);
    sc[i] = p;
    l += p;
  }
  bf16x8 v0 = lds_b128(kp + 2048), v1 = lds_b128(kp + 3072);
  o = MFMA32(v0, pack8(sc, 0), o);
  o = MFMA32(v1, pack8(sc, 1), o);
}

template <int QB>
DI void attn_early(lds_char* kb, bf16x8 q0, bf16x8 q1, int r, int h, AttnState& st) {
  f32x16 sc[QB + 1];
#pragma unroll
  for (int j = 0; j <= QB; ++j) sc[j] = qk_block(kb + (8 - j) * 8192, q0, q1);
#pragma unroll
  for (int i = 0; i < 16; ++i) {
    int key = (i & 3) + 8 * (i >> 2) + 4 * h;
    if (key > r) sc[0][i] = -INFINITY;
  }
  float m = -INFINITY;
#pragma unroll
  for (int j = 0; j <= QB; ++j) m = max16(m, sc[j]);
  m = xhalf_max(m);
  float l = 0.f;
  f32x16 o = {};
#pragma unroll
  for (int j = 0; j <= QB; ++j) pv_block(kb + (8 - j) * 8192, sc[j], m, l, o);
  st.o = o; st.m = m; st.l = l;
}
template <int N, bool MASK0>
DI void attn_late(lds_char* kb, bf16x8 q0, bf16x8 q1, int r, int h, AttnState& st) {
  f32x16 sc[N];
  float m = st.m;
#pragma unroll
  for (int j = 0; j < N; ++j) {
    sc[j] = qk_block(kb + j * 8192, q0, q1);
    if (MASK0 && j == 0) {
#pragma unroll
      for (int i = 0; i < 16; ++i) {
        int key = (i & 3) + 8 * (i >> 2) + 4 * h;
        if (!(key > r)) sc[0][i] = -INFINITY;
      }
    }
    m = max16(m, sc[j]);
  }
  m = xhalf_max(m);
  const float alpha = __builtin_amdgcn_exp2f(st.m - m);
  float l = st.l * alpha;
  f32x16 o = st.o;
#pragma unroll
  for (int i = 0; i < 16; ++i) o[i] *= alpha;
#pragma unroll
  for (int j = 0; j < N; ++j) pv_block(kb + j * 8192, sc[j], m, l, o);
  st.o = o; st.m = m; st.l = l;
}

__global__ __launch_bounds__(512, 2) void k_fused(const __hip_bfloat16* __restrict__ x, const __hip_bfloat16* __restrict__ w,
                                                  const __hip_bfloat16* __restrict__ wo, __hip_bfloat16* __restrict__ out,
                                                  char* __restrict__ ws) {
  extern __shared__ __attribute__((aligned(16))) char smem_[];
  lds_char* smem = (lds_char*)smem_;
  STAMP(0);
  const int tid = threadIdx.x, lane = tid & 63;
  const int wv = __builtin_amdgcn_readfirstlane(tid >> 6);
  const int b = blockIdx.x, xcd = b & 7, idx = b >> 3;
  const int rb = (xcd >> 2) * 8 + (idx & 7);
  const int hp = (xcd & 3) * 4 + (idx >> 3);
  const int rbo = xcd * 2 + (idx & 1);
  const int cbo = idx >> 1;
  __amdgpu_buffer_rsrc_t rx = __builtin_amdgcn_make_buffer_rsrc((void*)x, 0, S_LEN * DM * 2, 0x00020000);
  __amdgpu_buffer_rsrc_t rw = __builtin_amdgcn_make_buffer_rsrc((void*)w, 0, DM * 3 * DM * 2, 0x00020000);
  __amdgpu_buffer_rsrc_t ro = __builtin_amdgcn_make_buffer_rsrc((void*)wo, 0, DM * DM * 2, 0x00020000);
  __amdgpu_buffer_rsrc_t rkv = __builtin_amdgcn_make_buffer_rsrc((void*)(ws + WS_KV), 0, 8u << 20, 0x00020000);
  __amdgpu_buffer_rsrc_t rof = __builtin_amdgcn_make_buffer_rsrc((void*)(ws + WS_OF), 0, 4u << 20, 0x00020000);
  gu64* epk = (gu64*)(ws + WS_EPK);
  gu64* epo = (gu64*)(ws + WS_EPO);
  gu64* flk = (gu64*)(ws + WS_FLK);
  gu64* flo = (gu64*)(ws + WS_FLO);

  const int lw = wv & 3;
  unsigned vx[4];
#pragma unroll
  for (int i = 0; i < 4; ++i) {
    int row = i * 8 + (lane >> 3);
    int f = (((row >> 4) & 1) << 2) | ((row >> 1) & 3);
    vx[i] = (unsigned)((rb * 128 + lw * 32 + row) * 2048 + (((lane & 7) ^ f) << 4));
  }
  const unsigned vwo = (unsigned)((lane >> 3) * 2048 + cbo * 128 + (((lane & 7) ^ (((lane >> 4) & 3) << 1)) << 4));

  u64 e_kv, e_o, e_mykv, e_myo;

  if (wv >= 4) {
    unsigned vw[6];
#pragma unroll
    for (int i = 0; i < 6; ++i) {
      int pi = 6 * lw + i, seg = pi >> 3, k = (pi & 7) * 8 + (lane >> 3);
      int lc = (lane & 7) ^ ((((k >> 1) & 1) | (((k >> 3) & 1) << 1)) << 1);
      vw[i] = (unsigned)(k * 6144 + seg * 2048 + hp * 128 + lc * 16);
    }
    auto stage = [&](int kt, int slot) {
      lds_char* base = smem + slot * STAGE;
#pragma unroll
      for (int i = 0; i < 4; ++i) glds16(rx, base + (4 * lw + i) * 1024, vx[i], kt * 128);
#pragma unroll
      for (int i = 0; i < 6; ++i) glds16(rw, base + XB + (6 * lw + i) * 1024, vw[i], kt * 64 * 6144);
    };
    auto wo_early = [&](int p) {
      int kh = p >= 24, row = kh * 512 + (p - kh * 24) * 8;
      glds16(ro, smem + L_WP + p * 1024, vwo, row * 2048);
    };
#pragma unroll
    for (int i = 0; i < 6; ++i) glds16(rw, smem + XB + (6 * lw + i) * 1024, vw[i], 0);
    stage(1, 1);
    WAIT_V(10); BAR();
    STAMP(1);
    e_kv = 0;
    e_o = __hip_atomic_load(epo + rb * 16 + (lane & 15), RLX_AGENT);
    e_mykv = __hip_atomic_load(epk + hp * 16 + rb, RLX_AGENT);
    e_myo = __hip_atomic_load(epo + rbo * 16 + cbo, RLX_AGENT);
    stage(2, 2);
    int slot = 0;
    for (int kt = 0; kt < 15; ++kt) {
      WAIT_V(10);
      BAR();
      if (kt < 13) stage(kt + 3, slot);
      else if (kt == 13) {
#pragma unroll
        for (int i = 0; i < 10; ++i) wo_early(8 + lw * 10 + i);
      } else {
        wo_early(lw * 2); wo_early(lw * 2 + 1);
      }
      slot = slot + 1 == 3 ? 0 : slot + 1;
    }
    STAMP(2);
  } else {
#pragma unroll
    for (int i = 0; i < 4; ++i) glds16(rx, smem + (4 * lw + i) * 1024, vx[i], 0);
    {
      int c = rb + 1 + (lane & 1);
      e_kv = __hip_atomic_load(epk + hp * 16 + (c > 15 ? 15 : c), RLX_AGENT);
    }
    e_o = __hip_atomic_load(epo + rb * 16 + (lane & 15), RLX_AGENT);
    e_mykv = __hip_atomic_load(epk + hp * 16 + rb, RLX_AGENT);
    e_myo = __hip_atomic_load(epo + rbo * 16 + cbo, RLX_AGENT);
    const int rp = wv & 1, hh = wv >> 1;
    const int r = lane & 15, g = lane >> 4;
    const int arow = (r & 7) + 16 * (r >> 3);
    const unsigned aoff = (rp * 64 + arow) * 128 + ((g ^ (((r >> 3) << 2) | ((r >> 1) & 3))) << 4);
    const int q4 = (lane & 15) >> 2, p4 = lane & 3;
    const int f = (q4 >> 1) | ((g & 1) << 1);
    const unsigned boff0 = XB + (8 * g + q4) * 128 + (((hh * 4 + 0 + (p4 >> 1)) ^ (f << 1)) << 4) + 8 * (p4 & 1);
    const unsigned boff1 = XB + (8 * g + q4) * 128 + (((hh * 4 + 2 + (p4 >> 1)) ^ (f << 1)) << 4) + 8 * (p4 & 1);
    f32x4 acc[4][6];
#pragma unroll
    for (int m = 0; m < 4; ++m)
#pragma unroll
      for (int n = 0; n < 6; ++n) acc[m][n] = f32x4{0, 0, 0, 0};
    WAIT_V(0); __builtin_amdgcn_s_waitcnt(0x0F70); BAR();
    STAMP(1);
    bf16x8 fa[2][4], fb[2][6];
    auto rd = [&](int set, lds_char* base, int s) {
      lds_char* b0 = base + boff0 + s * 4096; lds_char* b1 = base + boff1 + s * 4096;
      fb[set][0] = lds_tr2(b0, b0 + 512);
      fa[set][0] = lds_b128(base + (aoff ^ (s << 6)));
      fa[set][1] = lds_b128(base + (aoff ^ (s << 6)) + 1024);
      fa[set][2] = lds_b128(base + (aoff ^ (s << 6)) + 4096);
      fa[set][3] = lds_b128(base + (aoff ^ (s << 6)) + 5120);
      fb[set][1] = lds_tr2(b1, b1 + 512);
      fb[set][2] = lds_tr2(b0 + 8192, b0 + 8192 + 512);
      fb[set][3] = lds_tr2(b1 + 8192, b1 + 8192 + 512);
      fb[set][4] = lds_tr2(b0 + 16384, b0 + 16384 + 512);
      fb[set][5] = lds_tr2(b1 + 16384, b1 + 16384 + 512);
    };
    auto mm = [&](int set) {
#pragma unroll
      for (int n = 0; n < 6; ++n)
#pragma unroll
        for (int m = 0; m < 4; ++m)
          acc[m][n] = n < 4 ? MFMA16(fb[set][n], fa[set][m], acc[m][n]) : MFMA16(fa[set][m], fb[set][n], acc[m][n]);
    };
    rd(0, smem, 0);
    int slot = 0;
    for (int kt = 0; kt < 16; ++kt) {
      lds_char* base = smem + slot * STAGE;
      slot = slot + 1 == 3 ? 0 : slot + 1;
      lds_char* nbase = smem + slot * STAGE;
      SB0();
      rd(1, base, 1);
      mm(0);
#pragma unroll
      for (int i = 0; i < 16; ++i) { SGB(0x8, 1); SGB(0x100, 1); }
      SGB(0x8, 8);
      SB0();
      if (kt < 15) { WAIT_L0(); BAR(); }
      SB0();
      rd(0, nbase, 0);
      mm(1);
#pragma unroll
      for (int i = 0; i < 16; ++i) { SGB(0x8, 1); SGB(0x100, 1); }
      SGB(0x8, 8);
      SB0();
    }
    STAMP(2);
    const float qs = 0.17677669529663687f * 1.4426950408889634f;
    const unsigned gofs = (g >> 1) * 1024 + (g & 1) * 512;
    const unsigned kvo = (unsigned)((hp * 16 + rb) * 32768 + wv * 8192);
#pragma unroll
    for (int mp = 0; mp < 2; ++mp) {
      lds_char* dkv = smem + L_KVL + (rp * 2 + mp) * 8192 + hh * 4096;
#pragma unroll
      for (int mo = 0; mo < 2; ++mo) {
        const f32x4 a = acc[2 * mp + mo][2], c = acc[2 * mp + mo][3];
        u32x4 v = {pk2(a[0], a[1]), pk2(a[2], a[3]), pk2(c[0], c[1]), pk2(c[2], c[3])};
        const unsigned o = gofs + (arow + 8 * mo) * 16;
        if (rb < 15) __builtin_amdgcn_raw_buffer_store_b128(v, rkv, o + mp * 4096, kvo, 16);
        lds_st128(dkv + o, v);
      }
#pragma unroll
      for (int c2 = 0; c2 < 2; ++c2) {
        const f32x4 a = acc[2 * mp][4 + c2], c = acc[2 * mp + 1][4 + c2];
        u32x4 v = {pk2(a[0], a[1]), pk2(a[2], a[3]), pk2(c[0], c[1]), pk2(c[2], c[3])};
        const unsigned o = 2048 + gofs + (r + 16 * c2) * 16;
        if (rb < 15) __builtin_amdgcn_raw_buffer_store_b128(v, rkv, o + mp * 4096, kvo, 16);
        lds_st128(dkv + o, v);
      }
    }
#pragma unroll
    for (int m = 0; m < 4; ++m) {
      const f32x4 a = acc[m][0] * qs, c = acc[m][1] * qs;
      u32x4 v = {pk2(a[0], a[1]), pk2(a[2], a[3]), pk2(c[0], c[1]), pk2(c[2], c[3])};
      lds_st128(smem + L_Q + (hh * 4 + rp * 2 + (m >> 1)) * 2048 + gofs + (arow + 8 * (m & 1)) * 16, v);
    }
    WAIT_L0();
  }
  BAR();
  STAMP(3);

  const int ha = wv >> 2, qb = wv < 4 ? wv : 7 - wv;
  const int r32 = lane & 31, h2 = lane >> 5;
  const bf16x8 q0 = lds_b128(smem + L_Q + (ha * 4 + qb) * 2048 + lane * 16);
  const bf16x8 q1 = lds_b128(smem + L_Q + (ha * 4 + qb) * 2048 + 1024 + lane * 16);
  lds_char* kb = smem + qb * 8192 + ha * 4096 + lane * 16;
  AttnState st;
  if (qb == 0) attn_early<0>(kb, q0, q1, r32, h2, st);
  else if (qb == 1) attn_early<1>(kb, q0, q1, r32, h2, st);
  else if (qb == 2) attn_early<2>(kb, q0, q1, r32, h2, st);
  else attn_early<3>(kb, q0, q1, r32, h2, st);
  STAMP(4);
  if (wv < 4) {
    WAIT_V(0);
    int c = rb + 1 + lane;
    if (lane < 2 && c < 16) __hip_atomic_store(flk + (hp * 16 + c) * 16 + (1 - lane) * 4 + wv, e_kv + 1, RLX_AGENT);
  }
  if (rb > 0) {
    const int src = wv >> 2, prb = rb - 2 + src;
    if (prb >= 0) {
      wait_words(flk + (hp * 16 + rb) * 16 + wv, uni64(e_mykv) + 1);
      STAMP(5);
      u32x4 t[8];
#pragma unroll
      for (int i = 0; i < 8; ++i)
        t[i] = __builtin_amdgcn_raw_buffer_load_b128(rkv, lane * 16 + i * 1024, (hp * 16 + prb) * 32768 + (wv & 3) * 8192, 16);
      lds_char* d = smem + (src * 4 + (wv & 1) * 2) * 8192 + ((wv >> 1) & 1) * 4096 + lane * 16;
#pragma unroll
      for (int i = 0; i < 8; ++i) lds_st128(d + (i >> 2) * 8192 + (i & 3) * 1024, t[i]);
    }
    WAIT_L0();
    BAR();
    STAMP(6);
    if (tid == 0) __hip_atomic_store(epk + hp * 16 + rb, e_mykv + 1, RLX_AGENT);
    if (rb >= 2) {
      if (qb == 0) attn_late<8, true>(kb, q0, q1, r32, h2, st);
      else if (qb == 1) attn_late<7, true>(kb, q0, q1, r32, h2, st);
      else if (qb == 2) attn_late<6, true>(kb, q0, q1, r32, h2, st);
      else attn_late<5, true>(kb, q0, q1, r32, h2, st);
    } else {
      attn_late<4, false>(kb + (4 - qb) * 8192, q0, q1, r32, h2, st);
    }
  }
  {
    const float inv = 1.0f / xhalf_sum(st.l);
#pragma unroll
    for (int i = 0; i < 16; ++i) st.o[i] *= inv;
    const unsigned vo = (r32 >> 4) * 1024 + h2 * 256 + (r32 & 15) * 16;
    const unsigned so = (unsigned)(((rb * 4 + qb) * 32 + hp * 2 + ha) * 2048);
    __builtin_amdgcn_raw_buffer_store_b128(__builtin_bit_cast(u32x4, pack8(st.o, 0)), rof, vo, so, 16);
    __builtin_amdgcn_raw_buffer_store_b128(__builtin_bit_cast(u32x4, pack8(st.o, 1)), rof, vo + 512, so, 16);
    STAMP(7);
    WAIT_V(0);
    if (lane < 16)
      __hip_atomic_store(flo + ((rb * 16 + lane) * 8 + (hp >> 3) * 4 + qb) * 16 + (hp & 7) * 2 + ha, e_o + 1, RLX_AGENT);
  }
  BAR();

#pragma unroll
  for (int i = 0; i < 10; ++i) {
    const int p = wv * 10 + i, kh = wv >> 2;
    glds16(ro, smem + L_WL + p * 1024, vwo, (kh * 512 + 192 + (p - kh * 40) * 8) * 2048);
  }
  const int qo = wv & 3, kh = wv >> 2;
  wait_words(flo + ((rbo * 16 + cbo) * 8 + wv) * 16 + (lane & 15), uni64(e_myo) + 1);
  STAMP(8);
  u32x4 of[32];
#pragma unroll
  for (int i = 0; i < 32; ++i)
    of[i] = __builtin_amdgcn_raw_buffer_load_b128(rof, lane * 16 + (i & 3) * 1024, ((rbo * 4 + qo) * 32 + kh * 16) * 2048 + (i >> 2) * 4096, 16);
  BAR();
  if (tid == 0) __hip_atomic_store(epo + rbo * 16 + cbo, e_myo + 1, RLX_AGENT);
  f32x4 oc[2][4];
#pragma unroll
  for (int i = 0; i < 2; ++i)
#pragma unroll
    for (int c = 0; c < 4; ++c) oc[i][c] = f32x4{0, 0, 0, 0};
  {
    const int kg = lane >> 4, q4 = (lane & 15) >> 2, p4 = lane & 3;
    const int swz = 2 * (kg & 1) + (q4 >> 1);
    const unsigned rowo = (16 * (kg >> 1) + 4 * (kg & 1) + q4) * 128 + p4 * 8;
    bf16x8 wf[2][4];
    auto rdw = [&](int set, int i) {
      lds_char* base = i < 6 ? smem + L_WP + kh * 24576 + i * 4096 : smem + L_WL + kh * 40960 + (i - 6) * 4096;
#pragma unroll
      for (int c = 0; c < 4; ++c) {
        lds_char* p = base + rowo + ((c ^ swz) << 5);
        wf[set][c] = lds_tr2(p, p + 1024);
      }
    };
    rdw(0, 0);
#pragma unroll
    for (int i = 0; i < 16; ++i) {
      SB0();
      if (i < 15) rdw((i + 1) & 1, i + 1);
#pragma unroll
      for (int c = 0; c < 4; ++c)
#pragma unroll
        for (int q = 0; q < 2; ++q) oc[q][c] = MFMA16(wf[i & 1][c], __builtin_bit_cast(bf16x8, of[2 * i + q]), oc[q][c]);
#pragma unroll
      for (int k = 0; k < 8; ++k) { SGB(0x8, 1); SGB(0x100, 1); }
      SB0();
    }
  }
  STAMP(9);
  {
    lds_char* xp = smem + L_XC + wv * 4096 + lane * 16;
#pragma unroll
    for (int c = 0; c < 4; ++c) lds_st128(xp + c * 1024, __builtin_bit_cast(u32x4, kh ? oc[0][c] : oc[1][c]));
    WAIT_L0();
    BAR();
    lds_char* xq = smem + L_XC + (wv ^ 4) * 4096 + lane * 16;
    f32x4 fin[4];
#pragma unroll
    for (int c = 0; c < 4; ++c) fin[c] = (kh ? oc[1][c] : oc[0][c]) + *(__attribute__((address_space(3))) f32x4*)(xq + c * 1024);
    const int g4 = lane >> 4;
    char* dst = (char*)out + (size_t)(rbo * 128 + qo * 32 + kh * 16 + (lane & 15)) * 2048 + cbo * 128 + (g4 & 1) * 32 + (g4 >> 1) * 16;
#pragma unroll
    for (int u = 0; u < 2; ++u) {
      unsigned a0 = pk2(fin[2 * u][0], fin[2 * u][1]), a1 = pk2(fin[2 * u][2], fin[2 * u][3]);
      unsigned b0 = pk2(fin[2 * u + 1][0], fin[2 * u + 1][1]), b1 = pk2(fin[2 * u + 1][2], fin[2 * u + 1][3]);
      auto s0 = __builtin_amdgcn_permlane16_swap(a0, b0, false, false);
      auto s1 = __builtin_amdgcn_permlane16_swap(a1, b1, false, false);
      u32x4 v = {s0[0], s1[0], s0[1], s1[1]};
      *(u32x4*)(dst + u * 64) = v;
    }
  }
}

extern "C" void kernel_launch(void* const* d_in, const int* in_sizes, int n_in, void* d_out, int out_size,
                              void* d_ws, size_t ws_size, hipStream_t stream) {
  (void)hipFuncSetAttribute((const void*)k_fused, hipFuncAttributeMaxDynamicSharedMemorySize, LDS_BYTES);
  k_fused<<<256, 512, LDS_BYTES, stream>>>((const __hip_bfloat16*)d_in[0], (const __hip_bfloat16*)d_in[1],
                                           (const __hip_bfloat16*)d_in[2], (__hip_bfloat16*)d_out, (char*)d_ws);
}
```

```cpp
#include <hip/hip_runtime.h>
#include <hip/hip_bf16.h>
#include <stdint.h>

typedef __bf16 bf16x8 __attribute__((ext_vector_type(8)));
typedef __bf16 bf16x2 __attribute__((ext_vector_type(2)));
typedef short s16x4 __attribute__((ext_vector_type(4)));
typedef float f32x16 __attribute__((ext_vector_type(16)));
typedef float f32x4 __attribute__((ext_vector_type(4)));
typedef float f32x2 __attribute__((ext_vector_type(2)));
typedef unsigned u32x4 __attribute__((ext_vector_type(4)));
typedef unsigned u32x2 __attribute__((ext_vector_type(2)));
typedef __attribute__((address_space(3))) char lds_char;
typedef unsigned long long u64;
typedef __attribute__((address_space(1))) u64 gu64;

#define DI __device__ __forceinline__
#define MFMA32(a, b, c) __builtin_amdgcn_mfma_f32_32x32x16_bf16((a), (b), (c), 0, 0, 0)
#define MFMA16(a, b, c) __builtin_amdgcn_mfma_f32_16x16x32_bf16((a), (b), (c), 0, 0, 0)
#define WAIT_V(n) asm volatile("s_waitcnt vmcnt(" #n ")" ::: "memory")
#define WAIT_L0() __builtin_amdgcn_s_waitcnt(0xC07F)
#define BAR() __builtin_amdgcn_s_barrier()
#define SB0() __builtin_amdgcn_sched_barrier(0)
#define SGB(m, n) __builtin_amdgcn_sched_group_barrier((m), (n), 0)
#define RLX_AGENT __ATOMIC_RELAXED, __HIP_MEMORY_SCOPE_AGENT

constexpr int S_LEN = 2048, DM = 1024;

constexpr size_t WS_EPK = 0;
constexpr size_t WS_EPO = 2048;
constexpr size_t WS_FLK = 4096;
constexpr size_t WS_FLO = 65536;
constexpr size_t WS_KV = 1u << 20;
constexpr size_t WS_OF = WS_KV + (8u << 20);
constexpr size_t WS_ST = WS_OF + (4u << 20);

constexpr int STAGE = 40960, XB = 16384;
constexpr int L_KVT = 163840;
constexpr int L_Q = 49152;
constexpr int L_WP = 0;
constexpr int L_WL = 49152;
constexpr int L_XC = 131072;
constexpr int LDS_BYTES = 163840;
constexpr unsigned SPIN_CAP = 1u << 16;

#define GPOL ""
#define TPOL ""
#define RA 0
#define RB 0
#define PRO 0
#define TD 2
#define XT 1
#define WO0 3
DI unsigned pk2(float a, float b) {
  f32x2 v = {a, b};
  bf16x2 r = __builtin_convertvector(v, bf16x2);
  return __builtin_bit_cast(unsigned, r);
}
DI bf16x8 pack8(const f32x16& a, int s) {
  u32x4 r;
  r[0] = pk2(a[8 * s + 0], a[8 * s + 1]);
  r[1] = pk2(a[8 * s + 2], a[8 * s + 3]);
  r[2] = pk2(a[8 * s + 4], a[8 * s + 5]);
  r[3] = pk2(a[8 * s + 6], a[8 * s + 7]);
  return __builtin_bit_cast(bf16x8, r);
}
DI bf16x8 lds_b128(lds_char* p) { return *(__attribute__((address_space(3))) bf16x8*)p; }
DI void lds_st128(lds_char* p, u32x4 v) { *(__attribute__((address_space(3))) u32x4*)p = v; }
DI bf16x8 lds_tr2(lds_char* p0, lds_char* p1) {
  s16x4 a = __builtin_amdgcn_ds_read_tr16_b64_v4i16((__attribute__((address_space(3))) s16x4*)p0);
  s16x4 b = __builtin_amdgcn_ds_read_tr16_b64_v4i16((__attribute__((address_space(3))) s16x4*)p1);
  u32x2 ua = __builtin_bit_cast(u32x2, a), ub = __builtin_bit_cast(u32x2, b);
  u32x4 r = {ua[0], ua[1], ub[0], ub[1]};
  return __builtin_bit_cast(bf16x8, r);
}
DI void glds16(__amdgpu_buffer_rsrc_t rs, lds_char* dst, unsigned voff, unsigned soff) {
  unsigned m = (unsigned)(size_t)dst;
  asm volatile("s_mov_b32 m0, %0\n\ts_nop 0\n\tbuffer_load_dwordx4 %1, %2, %3 offen" GPOL " lds"
               :: "s"(m), "v"(voff), "s"(rs), "s"(soff) : "memory");
}
DI void touch(__amdgpu_buffer_rsrc_t rs, unsigned voff, unsigned soff) {
  asm volatile("buffer_load_dword v250, %0, %1, %2 offen" TPOL :: "v"(voff), "s"(rs), "s"(soff) : "memory", "v250");
}
template <int IMM>
DI void stouch(__amdgpu_buffer_rsrc_t rs, unsigned soff) {
  asm volatile("s_buffer_load_dword s99, %0, %1 offset:%2" :: "s"(rs), "s"(soff), "n"(IMM) : "memory", "s99");
}
DI float xhalf_max(float v) {
  auto rr = __builtin_amdgcn_permlane32_swap(__float_as_uint(v), __float_as_uint(v), false, false);
  return fmaxf(__uint_as_float(rr[0]), __uint_as_float(rr[1]));
}
DI float xhalf_sum(float v) {
  auto rr = __builtin_amdgcn_permlane32_swap(__float_as_uint(v), __float_as_uint(v), false, false);
  return __uint_as_float(rr[0]) + __uint_as_float(rr[1]);
}
DI float max16(float m, const f32x16& a) {
#pragma unroll
  for (int i = 0; i < 16; i += 2) m = fmaxf(fmaxf(m, a[i]), a[i + 1]);
  return m;
}
DI void wait_words(gu64* p, u64 want) {
#pragma unroll 1
  for (unsigned spins = 0; spins < SPIN_CAP; ++spins) {
    u64 v = __hip_atomic_load(p, RLX_AGENT);
    if (__all(v == want)) break;
    __builtin_amdgcn_s_sleep(1);
  }
  __builtin_amdgcn_fence(__ATOMIC_ACQUIRE, "wavefront");
}
DI u64 uni64(u64 v) {
  unsigned lo = __builtin_amdgcn_readfirstlane((unsigned)v), hi = __builtin_amdgcn_readfirstlane((unsigned)(v >> 32));
  return ((u64)hi << 32) | lo;
}

struct AttnState { f32x16 o; float m, l; };

DI f32x16 qk_block(lds_char* kp, bf16x8 q0, bf16x8 q1) {
  f32x16 z = {};
  bf16x8 k0 = lds_b128(kp), k1 = lds_b128(kp + 1024);
  z = MFMA32(k0, q0, z);
  z = MFMA32(k1, q1, z);
  return z;
}
DI void pv_block(lds_char* kp, f32x16& sc, float m, float& l, f32x16& o) {
#pragma unroll
  for (int i = 0; i < 16; ++i) {
    float p = __builtin_amdgcn_exp2f(sc[i] - m);
    sc[i] = p;
    l += p;
  }
  bf16x8 v0 = lds_b128(kp + 2048), v1 = lds_b128(kp + 3072);
  o = MFMA32(v0, pack8(sc, 0), o);
  o = MFMA32(v1, pack8(sc, 1), o);
}

template <int QB>
DI void attn_early(lds_char* kb, bf16x8 q0, bf16x8 q1, int r, int h, AttnState& st) {
  f32x16 sc[QB + 1];
#pragma unroll
  for (int j = 0; j <= QB; ++j) sc[j] = qk_block(kb + j * 8192, q0, q1);
#pragma unroll
  for (int i = 0; i < 16; ++i) {
    int key = (i & 3) + 8 * (i >> 2) + 4 * h;
    if (key > r) sc[0][i] = -INFINITY;
  }
  float m = -INFINITY;
#pragma unroll
  for (int j = 0; j <= QB; ++j) m = max16(m, sc[j]);
  m = xhalf_max(m);
  float l = 0.f;
  f32x16 o = {};
#pragma unroll
  for (int j = 0; j <= QB; ++j) pv_block(kb + j * 8192, sc[j], m, l, o);
  st.o = o; st.m = m; st.l = l;
}
template <int N, bool MASK0>
DI void attn_late(lds_char* kb, bf16x8 q0, bf16x8 q1, int r, int h, AttnState& st) {
  f32x16 sc[N];
  float m = st.m;
#pragma unroll
  for (int j = 0; j < N; ++j) {
    sc[j] = qk_block(kb + (N - 1 - j) * 8192, q0, q1);
    if (MASK0 && j == 0) {
#pragma unroll
      for (int i = 0; i < 16; ++i) {
        int key = (i & 3) + 8 * (i >> 2) + 4 * h;
        if (!(key > r)) sc[0][i] = -INFINITY;
      }
    }
    m = max16(m, sc[j]);
  }
  m = xhalf_max(m);
  const float alpha = __builtin_amdgcn_exp2f(st.m - m);
  float l = st.l * alpha;
  f32x16 o = st.o;
#pragma unroll
  for (int i = 0; i < 16; ++i) o[i] *= alpha;
#pragma unroll
  for (int j = 0; j < N; ++j) pv_block(kb + (N - 1 - j) * 8192, sc[j], m, l, o);
  st.o = o; st.m = m; st.l = l;
}

__global__ __launch_bounds__(512, 2) void k_fused(const __hip_bfloat16* __restrict__ x, const __hip_bfloat16* __restrict__ w,
                                                  const __hip_bfloat16* __restrict__ wo, __hip_bfloat16* __restrict__ out,
                                                  char* __restrict__ ws) {
  extern __shared__ __attribute__((aligned(16))) char smem_[];
  lds_char* smem = (lds_char*)smem_;
  const int tid = threadIdx.x, lane = tid & 63;
  const int wv = __builtin_amdgcn_readfirstlane(tid >> 6);
  const int b = blockIdx.x, xcd = b & 7, idx = b >> 3;
  const int rb = (xcd >> 2) * 8 + (idx & 7);
  const int hp = (xcd & 3) * 4 + (idx >> 3);
  const int rbo = xcd * 2 + (idx & 1);
  const int rot = (RA * (xcd & 3) + RB * (xcd >> 2)) & 15;
  const int cbo = idx >> 1;
  __amdgpu_buffer_rsrc_t rx = __builtin_amdgcn_make_buffer_rsrc((void*)x, 0, S_LEN * DM * 2, 0x00020000);
  __amdgpu_buffer_rsrc_t rw = __builtin_amdgcn_make_buffer_rsrc((void*)w, 0, DM * 3 * DM * 2, 0x00020000);
  __amdgpu_buffer_rsrc_t ro = __builtin_amdgcn_make_buffer_rsrc((void*)wo, 0, DM * DM * 2, 0x00020000);
  __amdgpu_buffer_rsrc_t rkv = __builtin_amdgcn_make_buffer_rsrc((void*)(ws + WS_KV), 0, 8u << 20, 0x00020000);
  __amdgpu_buffer_rsrc_t rof = __builtin_amdgcn_make_buffer_rsrc((void*)(ws + WS_OF), 0, 4u << 20, 0x00020000);
  u64 pc0; int clen;
  asm volatile("s_getpc_b64 %0\n.Lsw_pc0:\n\ts_mov_b32 %1, .Lsw_end-.Lsw_pc0" : "=s"(pc0), "=s"(clen));
  __amdgpu_buffer_rsrc_t rc = __builtin_amdgcn_make_buffer_rsrc((void*)pc0, 0, clen > 0 ? clen : 0, 0x00020000);
  gu64* epk = (gu64*)(ws + WS_EPK);
  gu64* epo = (gu64*)(ws + WS_EPO);
  gu64* flk = (gu64*)(ws + WS_FLK);
  gu64* flo = (gu64*)(ws + WS_FLO);

  const int lw = wv & 3;
  unsigned vx[4];
#pragma unroll
  for (int i = 0; i < 4; ++i) {
    int row = i * 8 + (lane >> 3);
    int f = (((row >> 4) & 1) << 2) | ((row >> 1) & 3);
    vx[i] = (unsigned)((rb * 128 + lw * 32 + row) * 2048 + (((lane & 7) ^ f) << 4));
  }
  const unsigned vwo = (unsigned)((lane >> 3) * 2048 + cbo * 128 + (((lane & 7) ^ (((lane >> 4) & 3) << 1)) << 4));

  u64 e_kv, e_o, e_mykv, e_myo;

  if (wv >= 4) {
    unsigned vw[6];
#pragma unroll
    for (int i = 0; i < 6; ++i) {
      int pi = 6 * lw + i, seg = pi >> 3, k = (pi & 7) * 8 + (lane >> 3);
      int lc = (lane & 7) ^ ((((k >> 1) & 1) | (((k >> 3) & 1) << 1)) << 1);
      vw[i] = (unsigned)(k * 6144 + seg * 2048 + hp * 128 + lc * 16);
    }
    auto stage = [&](int t, int slot) {
      const int kt = (t + rot) & 15;
      lds_char* base = smem + slot * STAGE;
#pragma unroll
      for (int i = 0; i < 4; ++i) glds16(rx, base + (4 * lw + i) * 1024, vx[i], kt * 128);
#pragma unroll
      for (int i = 0; i < 6; ++i) glds16(rw, base + XB + (6 * lw + i) * 1024, vw[i], kt * 64 * 6144);
    };
    auto wo_early = [&](int p) {
      int kh = p >= 24, row = kh * 512 + (p - kh * 24) * 8;
      glds16(ro, smem + L_WP + p * 1024, vwo, row * 2048);
    };
    const int tg = lane >> 4, tl = lane & 15;
    const __amdgpu_buffer_rsrc_t rt_ = lw < 2 ? rw : rx;
    const unsigned tstep = lw < 2 ? 192 * 2048 : 128, hm = lw < 2 ? 3 : 0, tn = lw < 2 ? 12 : (XT ? 16 : 0);
    const unsigned tb = (lw < 2 ? (idx & 7) * 24 + lw * 12 : rb * 128 + (idx >> 3) * 32 + (lw - 2) * 16) * 2048 + (lw < 2 ? (hp & 12) * 128 : 0);
    const unsigned tv1 = lane < tn ? tb + tl * 2048 : 0x80000000u;
    auto warm = [&](int t0, int n) {
      const unsigned k = (t0 + tg + rot) & 15;
      touch(rt_, tg < n && tl < tn ? tb + tl * 2048 + k * tstep + ((hp + k) & hm) * 128 : 0x80000000u, 0);
    };
    const __amdgpu_buffer_rsrc_t rb_ = lw < 2 ? rx : rw;
    const unsigned bstep = lw < 2 ? 128 : 192 * 2048;
    const unsigned bx = (rb * 128 + lw * 64) * 2048, bw = ((idx & 7) * 24 + (lw - 2) * 12) * 2048 + (hp & 12) * 128;
    const unsigned bv = lw < 2 ? bx + lane * 2048 : (lane < 48 ? bw + (lane >> 2) * 2048 + (lane & 3) * 128 : 0x80000000u);
    auto warm1 = [&](int t) {
      const unsigned k = (t + rot) & 15;
      touch(rb_, (t < 16 && ((t ^ hp) & 3) == 0) ? bv : 0x80000000u, k * bstep);
    };
#pragma unroll
    for (int i = 0; i < 6; ++i) glds16(rw, smem + XB + (6 * lw + i) * 1024, vw[i], rot * 64 * 6144);
    stage(1, 1);
    warm(2, 4); warm(6, TD - 2);
    WAIT_V(12); BAR();
    stage(2, 2);
    e_kv = 0;
    stage(3, 3);
    stouch<0>(rc, (idx * 8 + lw * 2) * 128); stouch<64>(rc, (idx * 8 + lw * 2) * 128);
    stouch<128>(rc, (idx * 8 + lw * 2) * 128); stouch<192>(rc, (idx * 8 + lw * 2) * 128);
    for (int kt = 0; kt < 12; ++kt) {
      if (kt == 0) { if (PRO == 0) WAIT_V(22); else WAIT_V(21); } else if (kt == 1) WAIT_V(21); else if (kt == 2) WAIT_V(22); else WAIT_V(23);
      BAR();
      stage(kt + 4, kt & 3);
      warm1(kt + 4 + TD);
      if (kt >= WO0 && kt < WO0 + 8) {
        const unsigned o = b * 8192 + lw * 2048 + (kt - WO0) * 256;
        stouch<0>(ro, o); stouch<64>(ro, o); stouch<128>(ro, o); stouch<192>(ro, o);
      }
    }
    WAIT_V(23); BAR();
#pragma unroll
    for (int i = 0; i < 10; ++i) wo_early(lw * 10 + i);
    WAIT_V(22); BAR();
    wo_early(40 + lw * 2); wo_early(40 + lw * 2 + 1);
    WAIT_V(12); BAR();
    WAIT_L0();
    e_o = __hip_atomic_load(epo + rb * 16 + (lane & 15), RLX_AGENT);
    e_mykv = __hip_atomic_load(epk + hp * 16 + rb, RLX_AGENT);
    e_myo = __hip_atomic_load(epo + rbo * 16 + cbo, RLX_AGENT);
  } else {
#pragma unroll
    for (int i = 0; i < 4; ++i) glds16(rx, smem + (4 * lw + i) * 1024, vx[i], rot * 128);
    {
      int c = rb + 1 + (lane & 1);
      e_kv = __hip_atomic_load(epk + hp * 16 + (c > 15 ? 15 : c), RLX_AGENT);
    }
    e_o = __hip_atomic_load(epo + rb * 16 + (lane & 15), RLX_AGENT);
    e_mykv = __hip_atomic_load(epk + hp * 16 + rb, RLX_AGENT);
    e_myo = __hip_atomic_load(epo + rbo * 16 + cbo, RLX_AGENT);
    const int rp = wv & 1, hh = wv >> 1;
    const int r = lane & 15, g = lane >> 4;
    const int arow = (r & 7) + 16 * (r >> 3);
    const unsigned aoff = (rp * 64 + arow) * 128 + ((g ^ (((r >> 3) << 2) | ((r >> 1) & 3))) << 4);
    const int q4 = (lane & 15) >> 2, p4 = lane & 3;
    const int f = (q4 >> 1) | ((g & 1) << 1);
    const unsigned boff0 = XB + (8 * g + q4) * 128 + (((hh * 4 + 0 + (p4 >> 1)) ^ (f << 1)) << 4) + 8 * (p4 & 1);
    const unsigned boff1 = XB + (8 * g + q4) * 128 + (((hh * 4 + 2 + (p4 >> 1)) ^ (f << 1)) << 4) + 8 * (p4 & 1);
    f32x4 acc[4][6];
#pragma unroll
    for (int m = 0; m < 4; ++m)
#pragma unroll
      for (int n = 0; n < 6; ++n) acc[m][n] = f32x4{0, 0, 0, 0};
    WAIT_V(0); __builtin_amdgcn_s_waitcnt(0x0F70); BAR();
    bf16x8 fa[2][4], fb[2][6];
    auto rd = [&](int set, lds_char* base, int s) {
      lds_char* b0 = base + boff0 + s * 4096; lds_char* b1 = base + boff1 + s * 4096;
      fb[set][0] = lds_tr2(b0, b0 + 512);
      fa[set][0] = lds_b128(base + (aoff ^ (s << 6)));
      fa[set][1] = lds_b128(base + (aoff ^ (s << 6)) + 1024);
      fa[set][2] = lds_b128(base + (aoff ^ (s << 6)) + 4096);
      fa[set][3] = lds_b128(base + (aoff ^ (s << 6)) + 5120);
      fb[set][1] = lds_tr2(b1, b1 + 512);
      fb[set][2] = lds_tr2(b0 + 8192, b0 + 8192 + 512);
      fb[set][3] = lds_tr2(b1 + 8192, b1 + 8192 + 512);
      fb[set][4] = lds_tr2(b0 + 16384, b0 + 16384 + 512);
      fb[set][5] = lds_tr2(b1 + 16384, b1 + 16384 + 512);
    };
    auto mm = [&](int set) {
#pragma unroll
      for (int n = 0; n < 6; ++n)
#pragma unroll
        for (int m = 0; m < 4; ++m)
          acc[m][n] = n < 4 ? MFMA16(fb[set][n], fa[set][m], acc[m][n]) : MFMA16(fa[set][m], fb[set][n], acc[m][n]);
    };
    rd(0, smem, 0);
    for (int kt = 0; kt < 16; ++kt) {
      lds_char* base = smem + (kt & 3) * STAGE;
      lds_char* nbase = smem + ((kt + 1) & 3) * STAGE;
      SB0();
      rd(1, base, 1);
      mm(0);
#pragma unroll
      for (int i = 0; i < 16; ++i) { SGB(0x8, 1); SGB(0x100, 1); }
      SGB(0x8, 8);
      SB0();
      if (kt < 15) { WAIT_L0(); BAR(); }
      SB0();
      rd(0, nbase, 0);
      mm(1);
#pragma unroll
      for (int i = 0; i < 16; ++i) { SGB(0x8, 1); SGB(0x100, 1); }
      SGB(0x8, 8);
      SB0();
    }
    const float qs = 0.17677669529663687f * 1.4426950408889634f;
    const unsigned gofs = (g >> 1) * 1024 + (g & 1) * 512;
    const unsigned kvo = (unsigned)((hp * 16 + rb) * 32768 + wv * 8192);
#pragma unroll
    for (int mp = 0; mp < 2; ++mp) {
      lds_char* dkv = smem + L_KVT - (9 + rp * 2 + mp) * 8192 + hh * 4096;
#pragma unroll
      for (int mo = 0; mo < 2; ++mo) {
        const f32x4 a = acc[2 * mp + mo][2], c = acc[2 * mp + mo][3];
        u32x4 v = {pk2(a[0], a[1]), pk2(a[2], a[3]), pk2(c[0], c[1]), pk2(c[2], c[3])};
        const unsigned o = gofs + (arow + 8 * mo) * 16;
        if (rb < 15) __builtin_amdgcn_raw_buffer_store_b128(v, rkv, o + mp * 4096, kvo, 16);
        lds_st128(dkv + o, v);
      }
#pragma unroll
      for (int c2 = 0; c2 < 2; ++c2) {
        const f32x4 a = acc[2 * mp][4 + c2], c = acc[2 * mp + 1][4 + c2];
        u32x4 v = {pk2(a[0], a[1]), pk2(a[2], a[3]), pk2(c[0], c[1]), pk2(c[2], c[3])};
        const unsigned o = 2048 + gofs + (r + 16 * c2) * 16;
        if (rb < 15) __builtin_amdgcn_raw_buffer_store_b128(v, rkv, o + mp * 4096, kvo, 16);
        lds_st128(dkv + o, v);
      }
    }
#pragma unroll
    for (int m = 0; m < 4; ++m) {
      const f32x4 a = acc[m][0] * qs, c = acc[m][1] * qs;
      u32x4 v = {pk2(a[0], a[1]), pk2(a[2], a[3]), pk2(c[0], c[1]), pk2(c[2], c[3])};
      lds_st128(smem + L_Q + (hh * 4 + rp * 2 + (m >> 1)) * 2048 + gofs + (arow + 8 * (m & 1)) * 16, v);
    }
    WAIT_L0();
  }
  BAR();

  const int ha = wv >> 2, qb = wv < 4 ? wv : 7 - wv;
  const int r32 = lane & 31, h2 = lane >> 5;
  const bf16x8 q0 = lds_b128(smem + L_Q + (ha * 4 + qb) * 2048 + lane * 16);
  const bf16x8 q1 = lds_b128(smem + L_Q + (ha * 4 + qb) * 2048 + 1024 + lane * 16);
  lds_char* kb = smem + L_KVT - (qb + 9) * 8192 + ha * 4096 + lane * 16;
  AttnState st;
  if (qb == 0) attn_early<0>(kb, q0, q1, r32, h2, st);
  else if (qb == 1) attn_early<1>(kb, q0, q1, r32, h2, st);
  else if (qb == 2) attn_early<2>(kb, q0, q1, r32, h2, st);
  else attn_early<3>(kb, q0, q1, r32, h2, st);
  if (wv < 4) {
    WAIT_V(0);
    int c = rb + 1 + lane;
    if (lane < 2 && c < 16) __hip_atomic_store(flk + (hp * 16 + c) * 16 + (1 - lane) * 4 + wv, e_kv + 1, RLX_AGENT);
  }
  if (rb > 0) {
    const int src = wv >> 2, prb = rb - 2 + src;
    if (prb >= 0) {
      wait_words(flk + (hp * 16 + rb) * 16 + wv, uni64(e_mykv) + 1);
      u32x4 t[8];
#pragma unroll
      for (int i = 0; i < 8; ++i)
        t[i] = __builtin_amdgcn_raw_buffer_load_b128(rkv, lane * 16 + i * 1024, (hp * 16 + prb) * 32768 + (wv & 3) * 8192, 16);
      lds_char* d = smem + L_KVT - (src * 4 + (wv & 1) * 2 + 2) * 8192 + ((wv >> 1) & 1) * 4096 + lane * 16;
#pragma unroll
      for (int i = 0; i < 8; ++i) lds_st128(d + (1 - (i >> 2)) * 8192 + (i & 3) * 1024, t[i]);
    }
    WAIT_L0();
    BAR();
    if (tid == 0) __hip_atomic_store(epk + hp * 16 + rb, e_mykv + 1, RLX_AGENT);
    lds_char* kl = smem + L_KVT - 8 * 8192 + ha * 4096 + lane * 16;
    if (rb >= 2) {
      if (qb == 0) attn_late<8, true>(kl, q0, q1, r32, h2, st);
      else if (qb == 1) attn_late<7, true>(kl, q0, q1, r32, h2, st);
      else if (qb == 2) attn_late<6, true>(kl, q0, q1, r32, h2, st);
      else attn_late<5, true>(kl, q0, q1, r32, h2, st);
    } else {
      attn_late<4, false>(kl, q0, q1, r32, h2, st);
    }
  }
  {
    const float inv = 1.0f / xhalf_sum(st.l);
#pragma unroll
    for (int i = 0; i < 16; ++i) st.o[i] *= inv;
    const unsigned vo = (r32 >> 4) * 1024 + h2 * 256 + (r32 & 15) * 16;
    const unsigned so = (unsigned)(((rb * 4 + qb) * 32 + hp * 2 + ha) * 2048);
    __builtin_amdgcn_raw_buffer_store_b128(__builtin_bit_cast(u32x4, pack8(st.o, 0)), rof, vo, so, 16);
    __builtin_amdgcn_raw_buffer_store_b128(__builtin_bit_cast(u32x4, pack8(st.o, 1)), rof, vo + 512, so, 16);
    WAIT_V(0);
    if (lane < 16)
      __hip_atomic_store(flo + ((rb * 16 + lane) * 8 + (hp >> 3) * 4 + qb) * 16 + (hp & 7) * 2 + ha, e_o + 1, RLX_AGENT);
  }
  BAR();

#pragma unroll
  for (int i = 0; i < 10; ++i) {
    const int p = wv * 10 + i, kh = wv >> 2;
    glds16(ro, smem + L_WL + p * 1024, vwo, (kh * 512 + 192 + (p - kh * 40) * 8) * 2048);
  }
  const int qo = wv & 3, kh = wv >> 2;
  wait_words(flo + ((rbo * 16 + cbo) * 8 + wv) * 16 + (lane & 15), uni64(e_myo) + 1);
  u32x4 of[32];
#pragma unroll
  for (int i = 0; i < 32; ++i)
    of[i] = __builtin_amdgcn_raw_buffer_load_b128(rof, lane * 16 + (i & 3) * 1024, ((rbo * 4 + qo) * 32 + kh * 16) * 2048 + (i >> 2) * 4096, 16);
  BAR();
  if (tid == 0) __hip_atomic_store(epo + rbo * 16 + cbo, e_myo + 1, RLX_AGENT);
  f32x4 oc[2][4];
#pragma unroll
  for (int i = 0; i < 2; ++i)
#pragma unroll
    for (int c = 0; c < 4; ++c) oc[i][c] = f32x4{0, 0, 0, 0};
  {
    const int kg = lane >> 4, q4 = (lane & 15) >> 2, p4 = lane & 3;
    const int swz = 2 * (kg & 1) + (q4 >> 1);
    const unsigned rowo = (16 * (kg >> 1) + 4 * (kg & 1) + q4) * 128 + p4 * 8;
    bf16x8 wf[2][4];
    auto rdw = [&](int set, int i) {
      lds_char* base = i < 6 ? smem + L_WP + kh * 24576 + i * 4096 : smem + L_WL + kh * 40960 + (i - 6) * 4096;
#pragma unroll
      for (int c = 0; c < 4; ++c) {
        lds_char* p = base + rowo + ((c ^ swz) << 5);
        wf[set][c] = lds_tr2(p, p + 1024);
      }
    };
    rdw(0, 0);
#pragma unroll
    for (int i = 0; i < 16; ++i) {
      SB0();
      if (i < 15) rdw((i + 1) & 1, i + 1);
#pragma unroll
      for (int c = 0; c < 4; ++c)
#pragma unroll
        for (int q = 0; q < 2; ++q) oc[q][c] = MFMA16(wf[i & 1][c], __builtin_bit_cast(bf16x8, of[2 * i + q]), oc[q][c]);
#pragma unroll
      for (int k = 0; k < 8; ++k) { SGB(0x8, 1); SGB(0x100, 1); }
      SB0();
    }
  }
  {
    lds_char* xp = smem + L_XC + wv * 4096 + lane * 16;
#pragma unroll
    for (int c = 0; c < 4; ++c) lds_st128(xp + c * 1024, __builtin_bit_cast(u32x4, kh ? oc[0][c] : oc[1][c]));
    WAIT_L0();
    BAR();
    lds_char* xq = smem + L_XC + (wv ^ 4) * 4096 + lane * 16;
    f32x4 fin[4];
#pragma unroll
    for (int c = 0; c < 4; ++c) fin[c] = (kh ? oc[1][c] : oc[0][c]) + *(__attribute__((address_space(3))) f32x4*)(xq + c * 1024);
    const int g4 = lane >> 4;
    char* dst = (char*)out + (size_t)(rbo * 128 + qo * 32 + kh * 16 + (lane & 15)) * 2048 + cbo * 128 + (g4 & 1) * 32 + (g4 >> 1) * 16;
#pragma unroll
    for (int u = 0; u < 2; ++u) {
      unsigned a0 = pk2(fin[2 * u][0], fin[2 * u][1]), a1 = pk2(fin[2 * u][2], fin[2 * u][3]);
      unsigned b0 = pk2(fin[2 * u + 1][0], fin[2 * u + 1][1]), b1 = pk2(fin[2 * u + 1][2], fin[2 * u + 1][3]);
      auto s0 = __builtin_amdgcn_permlane16_swap(a0, b0, false, false);
      auto s1 = __builtin_amdgcn_permlane16_swap(a1, b1, false, false);
      u32x4 v = {s0[0], s1[0], s0[1], s1[1]};
      *(u32x4*)(dst + u * 64) = v;
    }
  }
  asm volatile(".Lsw_end:");
}

extern "C" void kernel_launch(void* const* d_in, const int* in_sizes, int n_in, void* d_out, int out_size,
                              void* d_ws, size_t ws_size, hipStream_t stream) {
  (void)hipFuncSetAttribute((const void*)k_fused, hipFuncAttributeMaxDynamicSharedMemorySize, LDS_BYTES);
  k_fused<<<256, 512, LDS_BYTES, stream>>>((const __hip_bfloat16*)d_in[0], (const __hip_bfloat16*)d_in[1],
                                           (const __hip_bfloat16*)d_in[2], (__hip_bfloat16*)d_out, (char*)d_ws);
}
```

```cpp
#define PADAT 1
#define PADUS 10
#include <hip/hip_runtime.h>
#include <hip/hip_bf16.h>
#include <stdint.h>

typedef __bf16 bf16x8 __attribute__((ext_vector_type(8)));
typedef __bf16 bf16x2 __attribute__((ext_vector_type(2)));
typedef short s16x4 __attribute__((ext_vector_type(4)));
typedef float f32x16 __attribute__((ext_vector_type(16)));
typedef float f32x4 __attribute__((ext_vector_type(4)));
typedef float f32x2 __attribute__((ext_vector_type(2)));
typedef unsigned u32x4 __attribute__((ext_vector_type(4)));
typedef unsigned u32x2 __attribute__((ext_vector_type(2)));
typedef __attribute__((address_space(3))) char lds_char;
typedef unsigned long long u64;
typedef __attribute__((address_space(1))) u64 gu64;

#define DI __device__ __forceinline__
#define MFMA32(a, b, c) __builtin_amdgcn_mfma_f32_32x32x16_bf16((a), (b), (c), 0, 0, 0)
#define MFMA16(a, b, c) __builtin_amdgcn_mfma_f32_16x16x32_bf16((a), (b), (c), 0, 0, 0)
#define WAIT_V(n) asm volatile("s_waitcnt vmcnt(" #n ")" ::: "memory")
#define WAIT_L0() __builtin_amdgcn_s_waitcnt(0xC07F)
#define BAR() __builtin_amdgcn_s_barrier()
#define SB0() __builtin_amdgcn_sched_barrier(0)
#define SGB(m, n) __builtin_amdgcn_sched_group_barrier((m), (n), 0)
#define RLX_AGENT __ATOMIC_RELAXED, __HIP_MEMORY_SCOPE_AGENT

constexpr int S_LEN = 2048, DM = 1024;

constexpr size_t WS_EPK = 0;
constexpr size_t WS_EPO = 2048;
constexpr size_t WS_FLK = 4096;
constexpr size_t WS_FLO = 65536;
constexpr size_t WS_KV = 1u << 20;
constexpr size_t WS_OF = WS_KV + (8u << 20);
constexpr size_t WS_ST = WS_OF + (4u << 20);

constexpr int STAGE = 40960, XB = 16384;
constexpr int L_KVT = 163840;
constexpr int L_Q = 49152;
constexpr int L_WP = 0;
constexpr int L_WL = 49152;
constexpr int L_XC = 131072;
constexpr int LDS_BYTES = 163840;
constexpr unsigned SPIN_CAP = 1u << 16;

#define GPOL ""
#define TPOL ""
#define RA 0
#define RB 0
#define PRO 0
#define TD 2
#define XT 1
#define WO0 3
DI unsigned pk2(float a, float b) {
  f32x2 v = {a, b};
  bf16x2 r = __builtin_convertvector(v, bf16x2);
  return __builtin_bit_cast(unsigned, r);
}
DI bf16x8 pack8(const f32x16& a, int s) {
  u32x4 r;
  r[0] = pk2(a[8 * s + 0], a[8 * s + 1]);
  r[1] = pk2(a[8 * s + 2], a[8 * s + 3]);
  r[2] = pk2(a[8 * s + 4], a[8 * s + 5]);
  r[3] = pk2(a[8 * s + 6], a[8 * s + 7]);
  return __builtin_bit_cast(bf16x8, r);
}
DI bf16x8 lds_b128(lds_char* p) { return *(__attribute__((address_space(3))) bf16x8*)p; }
DI void lds_st128(lds_char* p, u32x4 v) { *(__attribute__((address_space(3))) u32x4*)p = v; }
DI bf16x8 lds_tr2(lds_char* p0, lds_char* p1) {
  s16x4 a = __builtin_amdgcn_ds_read_tr16_b64_v4i16((__attribute__((address_space(3))) s16x4*)p0);
  s16x4 b = __builtin_amdgcn_ds_read_tr16_b64_v4i16((__attribute__((address_space(3))) s16x4*)p1);
  u32x2 ua = __builtin_bit_cast(u32x2, a), ub = __builtin_bit_cast(u32x2, b);
  u32x4 r = {ua[0], ua[1], ub[0], ub[1]};
  return __builtin_bit_cast(bf16x8, r);
}
DI void glds16(__amdgpu_buffer_rsrc_t rs, lds_char* dst, unsigned voff, unsigned soff) {
  unsigned m = (unsigned)(size_t)dst;
  asm volatile("s_mov_b32 m0, %0\n\ts_nop 0\n\tbuffer_load_dwordx4 %1, %2, %3 offen" GPOL " lds"
               :: "s"(m), "v"(voff), "s"(rs), "s"(soff) : "memory");
}
DI void touch(__amdgpu_buffer_rsrc_t rs, unsigned voff, unsigned soff) {
  asm volatile("buffer_load_dword v250, %0, %1, %2 offen" TPOL :: "v"(voff), "s"(rs), "s"(soff) : "memory", "v250");
}
template <int IMM>
DI void stouch(__amdgpu_buffer_rsrc_t rs, unsigned soff) {
  asm volatile("s_buffer_load_dword s99, %0, %1 offset:%2" :: "s"(rs), "s"(soff), "n"(IMM) : "memory", "s99");
}
DI float xhalf_max(float v) {
  auto rr = __builtin_amdgcn_permlane32_swap(__float_as_uint(v), __float_as_uint(v), false, false);
  return fmaxf(__uint_as_float(rr[0]), __uint_as_float(rr[1]));
}
DI float xhalf_sum(float v) {
  auto rr = __builtin_amdgcn_permlane32_swap(__float_as_uint(v), __float_as_uint(v), false, false);
  return __uint_as_float(rr[0]) + __uint_as_float(rr[1]);
}
DI float max16(float m, const f32x16& a) {
#pragma unroll
  for (int i = 0; i < 16; i += 2) m = fmaxf(fmaxf(m, a[i]), a[i + 1]);
  return m;
}
DI void wait_words(gu64* p, u64 want) {
#pragma unroll 1
  for (unsigned spins = 0; spins < SPIN_CAP; ++spins) {
    u64 v = __hip_atomic_load(p, RLX_AGENT);
    if (__all(v == want)) break;
    __builtin_amdgcn_s_sleep(1);
  }
  __builtin_amdgcn_fence(__ATOMIC_ACQUIRE, "wavefront");
}
DI u64 uni64(u64 v) {
  unsigned lo = __builtin_amdgcn_readfirstlane((unsigned)v), hi = __builtin_amdgcn_readfirstlane((unsigned)(v >> 32));
  return ((u64)hi << 32) | lo;
}

DI u64 rtc() { u64 t; SB0(); asm volatile("s_memrealtime %0\n\ts_waitcnt lgkmcnt(0)" : "=s"(t) :: "memory"); SB0(); return t; }
#define STAMP(i) { if ((i) == 0) pad0 = rtc(); else if ((i) == PADAT) { for (int s_ = 0; s_ < 100000 && rtc() - pad0 < PADUS * 100; ++s_) __builtin_amdgcn_s_sleep(1); } }
#define STAMPK(k)

struct AttnState { f32x16 o; float m, l; };

DI f32x16 qk_block(lds_char* kp, bf16x8 q0, bf16x8 q1) {
  f32x16 z = {};
  bf16x8 k0 = lds_b128(kp), k1 = lds_b128(kp + 1024);
  z = MFMA32(k0, q0, z);
  z = MFMA32(k1, q1, z);
  return z;
}
DI void pv_block(lds_char* kp, f32x16& sc, float m, float& l, f32x16& o) {
#pragma unroll
  for (int i = 0; i < 16; ++i) {
    float p = __builtin_amdgcn_exp2f(sc[i] - m);
    sc[i] = p;
    l += p;
  }
  bf16x8 v0 = lds_b128(kp + 2048), v1 = lds_b128(kp + 3072);
  o = MFMA32(v0, pack8(sc, 0), o);
  o = MFMA32(v1, pack8(sc, 1), o);
}

template <int QB>
DI void attn_early(lds_char* kb, bf16x8 q0, bf16x8 q1, int r, int h, AttnState& st) {
  f32x16 sc[QB + 1];
#pragma unroll
  for (int j = 0; j <= QB; ++j) sc[j] = qk_block(kb + j * 8192, q0, q1);
#pragma unroll
  for (int i = 0; i < 16; ++i) {
    int key = (i & 3) + 8 * (i >> 2) + 4 * h;
    if (key > r) sc[0][i] = -INFINITY;
  }
  float m = -INFINITY;
#pragma unroll
  for (int j = 0; j <= QB; ++j) m = max16(m, sc[j]);
  m = xhalf_max(m);
  float l = 0.f;
  f32x16 o = {};
#pragma unroll
  for (int j = 0; j <= QB; ++j) pv_block(kb + j * 8192, sc[j], m, l, o);
  st.o = o; st.m = m; st.l = l;
}
template <int N, bool MASK0>
DI void attn_late(lds_char* kb, bf16x8 q0, bf16x8 q1, int r, int h, AttnState& st) {
  f32x16 sc[N];
  float m = st.m;
#pragma unroll
  for (int j = 0; j < N; ++j) {
    sc[j] = qk_block(kb + (N - 1 - j) * 8192, q0, q1);
    if (MASK0 && j == 0) {
#pragma unroll
      for (int i = 0; i < 16; ++i) {
        int key = (i & 3) + 8 * (i >> 2) + 4 * h;
        if (!(key > r)) sc[0][i] = -INFINITY;
      }
    }
    m = max16(m, sc[j]);
  }
  m = xhalf_max(m);
  const float alpha = __builtin_amdgcn_exp2f(st.m - m);
  float l = st.l * alpha;
  f32x16 o = st.o;
#pragma unroll
  for (int i = 0; i < 16; ++i) o[i] *= alpha;
#pragma unroll
  for (int j = 0; j < N; ++j) pv_block(kb + (N - 1 - j) * 8192, sc[j], m, l, o);
  st.o = o; st.m = m; st.l = l;
}

__global__ __launch_bounds__(512, 2) void k_fused(const __hip_bfloat16* __restrict__ x, const __hip_bfloat16* __restrict__ w,
                                                  const __hip_bfloat16* __restrict__ wo, __hip_bfloat16* __restrict__ out,
                                                  char* __restrict__ ws) {
  extern __shared__ __attribute__((aligned(16))) char smem_[];
  lds_char* smem = (lds_char*)smem_;
  u64 pad0;
  STAMP(0);
  const int tid = threadIdx.x, lane = tid & 63;
  const int wv = __builtin_amdgcn_readfirstlane(tid >> 6);
  const int b = blockIdx.x, xcd = b & 7, idx = b >> 3;
  const int rb = (xcd >> 2) * 8 + (idx & 7);
  const int hp = (xcd & 3) * 4 + (idx >> 3);
  const int rbo = xcd * 2 + (idx & 1);
  const int rot = (RA * (xcd & 3) + RB * (xcd >> 2)) & 15;
  const int cbo = idx >> 1;
  __amdgpu_buffer_rsrc_t rx = __builtin_amdgcn_make_buffer_rsrc((void*)x, 0, S_LEN * DM * 2, 0x00020000);
  __amdgpu_buffer_rsrc_t rw = __builtin_amdgcn_make_buffer_rsrc((void*)w, 0, DM * 3 * DM * 2, 0x00020000);
  __amdgpu_buffer_rsrc_t ro = __builtin_amdgcn_make_buffer_rsrc((void*)wo, 0, DM * DM * 2, 0x00020000);
  __amdgpu_buffer_rsrc_t rkv = __builtin_amdgcn_make_buffer_rsrc((void*)(ws + WS_KV), 0, 8u << 20, 0x00020000);
  __amdgpu_buffer_rsrc_t rof = __builtin_amdgcn_make_buffer_rsrc((void*)(ws + WS_OF), 0, 4u << 20, 0x00020000);
  u64 pc0; int clen;
  asm volatile("s_getpc_b64 %0\n.Lsw_pc0:\n\ts_mov_b32 %1, .Lsw_end-.Lsw_pc0" : "=s"(pc0), "=s"(clen));
  __amdgpu_buffer_rsrc_t rc = __builtin_amdgcn_make_buffer_rsrc((void*)pc0, 0, clen > 0 ? clen : 0, 0x00020000);
  gu64* epk = (gu64*)(ws + WS_EPK);
  gu64* epo = (gu64*)(ws + WS_EPO);
  gu64* flk = (gu64*)(ws + WS_FLK);
  gu64* flo = (gu64*)(ws + WS_FLO);

  const int lw = wv & 3;
  unsigned vx[4];
#pragma unroll
  for (int i = 0; i < 4; ++i) {
    int row = i * 8 + (lane >> 3);
    int f = (((row >> 4) & 1) << 2) | ((row >> 1) & 3);
    vx[i] = (unsigned)((rb * 128 + lw * 32 + row) * 2048 + (((lane & 7) ^ f) << 4));
  }
  const unsigned vwo = (unsigned)((lane >> 3) * 2048 + cbo * 128 + (((lane & 7) ^ (((lane >> 4) & 3) << 1)) << 4));

  u64 e_kv, e_o, e_mykv, e_myo;

  if (wv >= 4) {
    unsigned vw[6];
#pragma unroll
    for (int i = 0; i < 6; ++i) {
      int pi = 6 * lw + i, seg = pi >> 3, k = (pi & 7) * 8 + (lane >> 3);
      int lc = (lane & 7) ^ ((((k >> 1) & 1) | (((k >> 3) & 1) << 1)) << 1);
      vw[i] = (unsigned)(k * 6144 + seg * 2048 + hp * 128 + lc * 16);
    }
    auto stage = [&](int t, int slot) {
      const int kt = (t + rot) & 15;
      lds_char* base = smem + slot * STAGE;
#pragma unroll
      for (int i = 0; i < 4; ++i) glds16(rx, base + (4 * lw + i) * 1024, vx[i], kt * 128);
#pragma unroll
      for (int i = 0; i < 6; ++i) glds16(rw, base + XB + (6 * lw + i) * 1024, vw[i], kt * 64 * 6144);
    };
    auto wo_early = [&](int p) {
      int kh = p >= 24, row = kh * 512 + (p - kh * 24) * 8;
      glds16(ro, smem + L_WP + p * 1024, vwo, row * 2048);
    };
    const int tg = lane >> 4, tl = lane & 15;
    const __amdgpu_buffer_rsrc_t rt_ = lw < 2 ? rw : rx;
    const unsigned tstep = lw < 2 ? 192 * 2048 : 128, hm = lw < 2 ? 3 : 0, tn = lw < 2 ? 12 : (XT ? 16 : 0);
    const unsigned tb = (lw < 2 ? (idx & 7) * 24 + lw * 12 : rb * 128 + (idx >> 3) * 32 + (lw - 2) * 16) * 2048 + (lw < 2 ? (hp & 12) * 128 : 0);
    const unsigned tv1 = lane < tn ? tb + tl * 2048 : 0x80000000u;
    auto warm = [&](int t0, int n) {
      const unsigned k = (t0 + tg + rot) & 15;
      touch(rt_, tg < n && tl < tn ? tb + tl * 2048 + k * tstep + ((hp + k) & hm) * 128 : 0x80000000u, 0);
    };
    const __amdgpu_buffer_rsrc_t rb_ = lw < 2 ? rx : rw;
    const unsigned bstep = lw < 2 ? 128 : 192 * 2048;
    const unsigned bx = (rb * 128 + lw * 64) * 2048, bw = ((idx & 7) * 24 + (lw - 2) * 12) * 2048 + (hp & 12) * 128;
    const unsigned bv = lw < 2 ? bx + lane * 2048 : (lane < 48 ? bw + (lane >> 2) * 2048 + (lane & 3) * 128 : 0x80000000u);
    auto warm1 = [&](int t) {
      const unsigned k = (t + rot) & 15;
      touch(rb_, (t < 16 && ((t ^ hp) & 3) == 0) ? bv : 0x80000000u, k * bstep);
    };
#pragma unroll
    for (int i = 0; i < 6; ++i) glds16(rw, smem + XB + (6 * lw + i) * 1024, vw[i], rot * 64 * 6144);
    stage(1, 1);
    warm(2, 4); warm(6, TD - 2);
    WAIT_V(12); BAR();
    stage(2, 2);
    e_kv = 0;
    stage(3, 3);
    stouch<0>(rc, (idx * 8 + lw * 2) * 128); stouch<64>(rc, (idx * 8 + lw * 2) * 128);
    stouch<128>(rc, (idx * 8 + lw * 2) * 128); stouch<192>(rc, (idx * 8 + lw * 2) * 128);
    for (int kt = 0; kt < 12; ++kt) {
      if (kt == 0) { if (PRO == 0) WAIT_V(22); else WAIT_V(21); } else if (kt == 1) WAIT_V(21); else if (kt == 2) WAIT_V(22); else WAIT_V(23);
      BAR();
      stage(kt + 4, kt & 3);
      warm1(kt + 4 + TD);
      if (kt >= WO0 && kt < WO0 + 8) {
        const unsigned o = b * 8192 + lw * 2048 + (kt - WO0) * 256;
        stouch<0>(ro, o); stouch<64>(ro, o); stouch<128>(ro, o); stouch<192>(ro, o);
      }
    }
    WAIT_V(23); BAR();
#pragma unroll
    for (int i = 0; i < 10; ++i) wo_early(lw * 10 + i);
    WAIT_V(22); BAR();
    wo_early(40 + lw * 2); wo_early(40 + lw * 2 + 1);
    WAIT_V(12); BAR();
    WAIT_L0();
    e_o = __hip_atomic_load(epo + rb * 16 + (lane & 15), RLX_AGENT);
    e_mykv = __hip_atomic_load(epk + hp * 16 + rb, RLX_AGENT);
    e_myo = __hip_atomic_load(epo + rbo * 16 + cbo, RLX_AGENT);
    STAMP(2);
  } else {
#pragma unroll
    for (int i = 0; i < 4; ++i) glds16(rx, smem + (4 * lw + i) * 1024, vx[i], rot * 128);
    {
      int c = rb + 1 + (lane & 1);
      e_kv = __hip_atomic_load(epk + hp * 16 + (c > 15 ? 15 : c), RLX_AGENT);
    }
    e_o = __hip_atomic_load(epo + rb * 16 + (lane & 15), RLX_AGENT);
    e_mykv = __hip_atomic_load(epk + hp * 16 + rb, RLX_AGENT);
    e_myo = __hip_atomic_load(epo + rbo * 16 + cbo, RLX_AGENT);
    const int rp = wv & 1, hh = wv >> 1;
    const int r = lane & 15, g = lane >> 4;
    const int arow = (r & 7) + 16 * (r >> 3);
    const unsigned aoff = (rp * 64 + arow) * 128 + ((g ^ (((r >> 3) << 2) | ((r >> 1) & 3))) << 4);
    const int q4 = (lane & 15) >> 2, p4 = lane & 3;
    const int f = (q4 >> 1) | ((g & 1) << 1);
    const unsigned boff0 = XB + (8 * g + q4) * 128 + (((hh * 4 + 0 + (p4 >> 1)) ^ (f << 1)) << 4) + 8 * (p4 & 1);
    const unsigned boff1 = XB + (8 * g + q4) * 128 + (((hh * 4 + 2 + (p4 >> 1)) ^ (f << 1)) << 4) + 8 * (p4 & 1);
    f32x4 acc[4][6];
#pragma unroll
    for (int m = 0; m < 4; ++m)
#pragma unroll
      for (int n = 0; n < 6; ++n) acc[m][n] = f32x4{0, 0, 0, 0};
    WAIT_V(0); __builtin_amdgcn_s_waitcnt(0x0F70); BAR();
    STAMP(1);
    bf16x8 fa[2][4], fb[2][6];
    auto rd = [&](int set, lds_char* base, int s) {
      lds_char* b0 = base + boff0 + s * 4096; lds_char* b1 = base + boff1 + s * 4096;
      fb[set][0] = lds_tr2(b0, b0 + 512);
      fa[set][0] = lds_b128(base + (aoff ^ (s << 6)));
      fa[set][1] = lds_b128(base + (aoff ^ (s << 6)) + 1024);
      fa[set][2] = lds_b128(base + (aoff ^ (s << 6)) + 4096);
      fa[set][3] = lds_b128(base + (aoff ^ (s << 6)) + 5120);
      fb[set][1] = lds_tr2(b1, b1 + 512);
      fb[set][2] = lds_tr2(b0 + 8192, b0 + 8192 + 512);
      fb[set][3] = lds_tr2(b1 + 8192, b1 + 8192 + 512);
      fb[set][4] = lds_tr2(b0 + 16384, b0 + 16384 + 512);
      fb[set][5] = lds_tr2(b1 + 16384, b1 + 16384 + 512);
    };
    auto mm = [&](int set) {
#pragma unroll
      for (int n = 0; n < 6; ++n)
#pragma unroll
        for (int m = 0; m < 4; ++m)
          acc[m][n] = n < 4 ? MFMA16(fb[set][n], fa[set][m], acc[m][n]) : MFMA16(fa[set][m], fb[set][n], acc[m][n]);
    };
    rd(0, smem, 0);
    for (int kt = 0; kt < 16; ++kt) {
      lds_char* base = smem + (kt & 3) * STAGE;
      lds_char* nbase = smem + ((kt + 1) & 3) * STAGE;
      SB0();
      rd(1, base, 1);
      mm(0);
#pragma unroll
      for (int i = 0; i < 16; ++i) { SGB(0x8, 1); SGB(0x100, 1); }
      SGB(0x8, 8);
      SB0();
      if (kt < 15) { WAIT_L0(); STAMPK(kt + 16); BAR(); STAMPK(kt); }
      SB0();
      rd(0, nbase, 0);
      mm(1);
#pragma unroll
      for (int i = 0; i < 16; ++i) { SGB(0x8, 1); SGB(0x100, 1); }
      SGB(0x8, 8);
      SB0();
    }
    STAMP(2);
    const float qs = 0.17677669529663687f * 1.4426950408889634f;
    const unsigned gofs = (g >> 1) * 1024 + (g & 1) * 512;
    const unsigned kvo = (unsigned)((hp * 16 + rb) * 32768 + wv * 8192);
#pragma unroll
    for (int mp = 0; mp < 2; ++mp) {
      lds_char* dkv = smem + L_KVT - (9 + rp * 2 + mp) * 8192 + hh * 4096;
#pragma unroll
      for (int mo = 0; mo < 2; ++mo) {
        const f32x4 a = acc[2 * mp + mo][2], c = acc[2 * mp + mo][3];
        u32x4 v = {pk2(a[0], a[1]), pk2(a[2], a[3]), pk2(c[0], c[1]), pk2(c[2], c[3])};
        const unsigned o = gofs + (arow + 8 * mo) * 16;
        if (rb < 15) __builtin_amdgcn_raw_buffer_store_b128(v, rkv, o + mp * 4096, kvo, 16);
        lds_st128(dkv + o, v);
      }
#pragma unroll
      for (int c2 = 0; c2 < 2; ++c2) {
        const f32x4 a = acc[2 * mp][4 + c2], c = acc[2 * mp + 1][4 + c2];
        u32x4 v = {pk2(a[0], a[1]), pk2(a[2], a[3]), pk2(c[0], c[1]), pk2(c[2], c[3])};
        const unsigned o = 2048 + gofs + (r + 16 * c2) * 16;
        if (rb < 15) __builtin_amdgcn_raw_buffer_store_b128(v, rkv, o + mp * 4096, kvo, 16);
        lds_st128(dkv + o, v);
      }
    }
#pragma unroll
    for (int m = 0; m < 4; ++m) {
      const f32x4 a = acc[m][0] * qs, c = acc[m][1] * qs;
      u32x4 v = {pk2(a[0], a[1]), pk2(a[2], a[3]), pk2(c[0], c[1]), pk2(c[2], c[3])};
      lds_st128(smem + L_Q + (hh * 4 + rp * 2 + (m >> 1)) * 2048 + gofs + (arow + 8 * (m & 1)) * 16, v);
    }
    WAIT_L0();
  }
  BAR();
  STAMP(3);

  const int ha = wv >> 2, qb = wv < 4 ? wv : 7 - wv;
  const int r32 = lane & 31, h2 = lane >> 5;
  const bf16x8 q0 = lds_b128(smem + L_Q + (ha * 4 + qb) * 2048 + lane * 16);
  const bf16x8 q1 = lds_b128(smem + L_Q + (ha * 4 + qb) * 2048 + 1024 + lane * 16);
  lds_char* kb = smem + L_KVT - (qb + 9) * 8192 + ha * 4096 + lane * 16;
  AttnState st;
  if (qb == 0) attn_early<0>(kb, q0, q1, r32, h2, st);
  else if (qb == 1) attn_early<1>(kb, q0, q1, r32, h2, st);
  else if (qb == 2) attn_early<2>(kb, q0, q1, r32, h2, st);
  else attn_early<3>(kb, q0, q1, r32, h2, st);
  STAMP(4);
  if (wv < 4) {
    WAIT_V(0);
    int c = rb + 1 + lane;
    if (lane < 2 && c < 16) __hip_atomic_store(flk + (hp * 16 + c) * 16 + (1 - lane) * 4 + wv, e_kv + 1, RLX_AGENT);
  }
  if (rb > 0) {
    const int src = wv >> 2, prb = rb - 2 + src;
    if (prb >= 0) {
      wait_words(flk + (hp * 16 + rb) * 16 + wv, uni64(e_mykv) + 1);
      STAMP(5);
      u32x4 t[8];
#pragma unroll
      for (int i = 0; i < 8; ++i)
        t[i] = __builtin_amdgcn_raw_buffer_load_b128(rkv, lane * 16 + i * 1024, (hp * 16 + prb) * 32768 + (wv & 3) * 8192, 16);
      lds_char* d = smem + L_KVT - (src * 4 + (wv & 1) * 2 + 2) * 8192 + ((wv >> 1) & 1) * 4096 + lane * 16;
#pragma unroll
      for (int i = 0; i < 8; ++i) lds_st128(d + (1 - (i >> 2)) * 8192 + (i & 3) * 1024, t[i]);
    }
    WAIT_L0();
    BAR();
    STAMP(6);
    if (tid == 0) __hip_atomic_store(epk + hp * 16 + rb, e_mykv + 1, RLX_AGENT);
    lds_char* kl = smem + L_KVT - 8 * 8192 + ha * 4096 + lane * 16;
    if (rb >= 2) {
      if (qb == 0) attn_late<8, true>(kl, q0, q1, r32, h2, st);
      else if (qb == 1) attn_late<7, true>(kl, q0, q1, r32, h2, st);
      else if (qb == 2) attn_late<6, true>(kl, q0, q1, r32, h2, st);
      else attn_late<5, true>(kl, q0, q1, r32, h2, st);
    } else {
      attn_late<4, false>(kl, q0, q1, r32, h2, st);
    }
  }
  {
    const float inv = 1.0f / xhalf_sum(st.l);
#pragma unroll
    for (int i = 0; i < 16; ++i) st.o[i] *= inv;
    const unsigned vo = (r32 >> 4) * 1024 + h2 * 256 + (r32 & 15) * 16;
    const unsigned so = (unsigned)(((rb * 4 + qb) * 32 + hp * 2 + ha) * 2048);
    __builtin_amdgcn_raw_buffer_store_b128(__builtin_bit_cast(u32x4, pack8(st.o, 0)), rof, vo, so, 16);
    __builtin_amdgcn_raw_buffer_store_b128(__builtin_bit_cast(u32x4, pack8(st.o, 1)), rof, vo + 512, so, 16);
    STAMP(7);
    WAIT_V(0);
    if (lane < 16)
      __hip_atomic_store(flo + ((rb * 16 + lane) * 8 + (hp >> 3) * 4 + qb) * 16 + (hp & 7) * 2 + ha, e_o + 1, RLX_AGENT);
  }
  BAR();

#pragma unroll
  for (int i = 0; i < 10; ++i) {
    const int p = wv * 10 + i, kh = wv >> 2;
    glds16(ro, smem + L_WL + p * 1024, vwo, (kh * 512 + 192 + (p - kh * 40) * 8) * 2048);
  }
  const int qo = wv & 3, kh = wv >> 2;
  wait_words(flo + ((rbo * 16 + cbo) * 8 + wv) * 16 + (lane & 15), uni64(e_myo) + 1);
  STAMP(8);
  u32x4 of[32];
#pragma unroll
  for (int i = 0; i < 32; ++i)
    of[i] = __builtin_amdgcn_raw_buffer_load_b128(rof, lane * 16 + (i & 3) * 1024, ((rbo * 4 + qo) * 32 + kh * 16) * 2048 + (i >> 2) * 4096, 16);
  BAR();
  if (tid == 0) __hip_atomic_store(epo + rbo * 16 + cbo, e_myo + 1, RLX_AGENT);
  f32x4 oc[2][4];
#pragma unroll
  for (int i = 0; i < 2; ++i)
#pragma unroll
    for (int c = 0; c < 4; ++c) oc[i][c] = f32x4{0, 0, 0, 0};
  {
    const int kg = lane >> 4, q4 = (lane & 15) >> 2, p4 = lane & 3;
    const int swz = 2 * (kg & 1) + (q4 >> 1);
    const unsigned rowo = (16 * (kg >> 1) + 4 * (kg & 1) + q4) * 128 + p4 * 8;
    bf16x8 wf[2][4];
    auto rdw = [&](int set, int i) {
      lds_char* base = i < 6 ? smem + L_WP + kh * 24576 + i * 4096 : smem + L_WL + kh * 40960 + (i - 6) * 4096;
#pragma unroll
      for (int c = 0; c < 4; ++c) {
        lds_char* p = base + rowo + ((c ^ swz) << 5);
        wf[set][c] = lds_tr2(p, p + 1024);
      }
    };
    rdw(0, 0);
#pragma unroll
    for (int i = 0; i < 16; ++i) {
      SB0();
      if (i < 15) rdw((i + 1) & 1, i + 1);
#pragma unroll
      for (int c = 0; c < 4; ++c)
#pragma unroll
        for (int q = 0; q < 2; ++q) oc[q][c] = MFMA16(wf[i & 1][c], __builtin_bit_cast(bf16x8, of[2 * i + q]), oc[q][c]);
#pragma unroll
      for (int k = 0; k < 8; ++k) { SGB(0x8, 1); SGB(0x100, 1); }
      SB0();
    }
  }
  STAMP(9);
  {
    lds_char* xp = smem + L_XC + wv * 4096 + lane * 16;
#pragma unroll
    for (int c = 0; c < 4; ++c) lds_st128(xp + c * 1024, __builtin_bit_cast(u32x4, kh ? oc[0][c] : oc[1][c]));
    WAIT_L0();
    BAR();
    lds_char* xq = smem + L_XC + (wv ^ 4) * 4096 + lane * 16;
    f32x4 fin[4];
#pragma unroll
    for (int c = 0; c < 4; ++c) fin[c] = (kh ? oc[1][c] : oc[0][c]) + *(__attribute__((address_space(3))) f32x4*)(xq + c * 1024);
    const int g4 = lane >> 4;
    char* dst = (char*)out + (size_t)(rbo * 128 + qo * 32 + kh * 16 + (lane & 15)) * 2048 + cbo * 128 + (g4 & 1) * 32 + (g4 >> 1) * 16;
#pragma unroll
    for (int u = 0; u < 2; ++u) {
      unsigned a0 = pk2(fin[2 * u][0], fin[2 * u][1]), a1 = pk2(fin[2 * u][2], fin[2 * u][3]);
      unsigned b0 = pk2(fin[2 * u + 1][0], fin[2 * u + 1][1]), b1 = pk2(fin[2 * u + 1][2], fin[2 * u + 1][3]);
      auto s0 = __builtin_amdgcn_permlane16_swap(a0, b0, false, false);
      auto s1 = __builtin_amdgcn_permlane16_swap(a1, b1, false, false);
      u32x4 v = {s0[0], s1[0], s0[1], s1[1]};
      *(u32x4*)(dst + u * 64) = v;
    }
  }
  asm volatile(".Lsw_end:");
  STAMP(10);
}

extern "C" void kernel_launch(void* const* d_in, const int* in_sizes, int n_in, void* d_out, int out_size,
                              void* d_ws, size_t ws_size, hipStream_t stream) {
  (void)hipFuncSetAttribute((const void*)k_fused, hipFuncAttributeMaxDynamicSharedMemorySize, LDS_BYTES);
  k_fused<<<256, 512, LDS_BYTES, stream>>>((const __hip_bfloat16*)d_in[0], (const __hip_bfloat16*)d_in[1],
                                           (const __hip_bfloat16*)d_in[2], (__hip_bfloat16*)d_out, (char*)d_ws);
}
```

```cpp
#include <hip/hip_runtime.h>
#include <hip/hip_bf16.h>
#include <stdint.h>

typedef __bf16 bf16x8 __attribute__((ext_vector_type(8)));
typedef __bf16 bf16x2 __attribute__((ext_vector_type(2)));
typedef short s16x4 __attribute__((ext_vector_type(4)));
typedef float f32x16 __attribute__((ext_vector_type(16)));
typedef float f32x4 __attribute__((ext_vector_type(4)));
typedef float f32x2 __attribute__((ext_vector_type(2)));
typedef unsigned u32x4 __attribute__((ext_vector_type(4)));
typedef unsigned u32x2 __attribute__((ext_vector_type(2)));
typedef __attribute__((address_space(3))) char lds_char;
typedef unsigned long long u64;
typedef __attribute__((address_space(1))) u64 gu64;

#define DI __device__ __forceinline__
#define MFMA32(a, b, c) __builtin_amdgcn_mfma_f32_32x32x16_bf16((a), (b), (c), 0, 0, 0)
#define MFMA16(a, b, c) __builtin_amdgcn_mfma_f32_16x16x32_bf16((a), (b), (c), 0, 0, 0)
#define WAIT_V(n) asm volatile("s_waitcnt vmcnt(" #n ")" ::: "memory")
#define WAIT_L0() __builtin_amdgcn_s_waitcnt(0xC07F)
#define WAIT_V0C() __builtin_amdgcn_s_waitcnt(0x0F70)
#define FRESH() asm volatile("" ::: "memory")
#define BAR() __builtin_amdgcn_s_barrier()
#define SB0() __builtin_amdgcn_sched_barrier(0)
#define SGB(m, n) __builtin_amdgcn_sched_group_barrier((m), (n), 0)
#define RLX_AGENT __ATOMIC_RELAXED, __HIP_MEMORY_SCOPE_AGENT

constexpr int S_LEN = 2048, DM = 1024;

constexpr size_t WS_EPK = 0;
constexpr size_t WS_EPO = 2048;
constexpr size_t WS_FLK = 65536;
constexpr size_t WS_FLO = 131072;
constexpr size_t WS_SYNC = WS_FLO + 256 * 16 * 128;
constexpr size_t WS_KV = 1u << 20;
constexpr size_t WS_OF = WS_KV + (8u << 20);
constexpr size_t WS_ST = WS_OF + (4u << 20);
constexpr unsigned OOB = 0x80000000u;

constexpr int STAGE = 40960, XB = 16384;
constexpr u64 KVPOS = 0x9843765ED210ull;
constexpr int L_Q0 = 13 * 8192, L_Q1 = 18 * 8192;
constexpr int L_WA0 = 10 * 8192, L_WA1 = 15 * 8192 - 6 * 4096;
constexpr int L_WB = 0;
constexpr int L_XC0 = 8 * 8192, L_XC1 = 13 * 8192;
constexpr int LDS_BYTES = 163840;
constexpr unsigned SPIN_CAP = 1u << 16;

#define GPOL ""
#define TPOL ""
#define RA 0
#define RB 0
#define PRO 0
#define TD 2
#define XT 1
#define WO0 3
#define WN0 8
#define WN1 12
#define OD 3
DI unsigned pk2(float a, float b) {
  f32x2 v = {a, b};
  bf16x2 r = __builtin_convertvector(v, bf16x2);
  return __builtin_bit_cast(unsigned, r);
}
DI bf16x8 pack8(const f32x16& a, int s) {
  u32x4 r;
  r[0] = pk2(a[8 * s + 0], a[8 * s + 1]);
  r[1] = pk2(a[8 * s + 2], a[8 * s + 3]);
  r[2] = pk2(a[8 * s + 4], a[8 * s + 5]);
  r[3] = pk2(a[8 * s + 6], a[8 * s + 7]);
  return __builtin_bit_cast(bf16x8, r);
}
DI bf16x8 lds_b128(lds_char* p) { return *(__attribute__((address_space(3))) bf16x8*)p; }
DI void lds_st128(lds_char* p, u32x4 v) { *(__attribute__((address_space(3))) u32x4*)p = v; }
DI bf16x8 lds_tr2(lds_char* p0, lds_char* p1) {
  s16x4 a = __builtin_amdgcn_ds_read_tr16_b64_v4i16((__attribute__((address_space(3))) s16x4*)p0);
  s16x4 b = __builtin_amdgcn_ds_read_tr16_b64_v4i16((__attribute__((address_space(3))) s16x4*)p1);
  u32x2 ua = __builtin_bit_cast(u32x2, a), ub = __builtin_bit_cast(u32x2, b);
  u32x4 r = {ua[0], ua[1], ub[0], ub[1]};
  return __builtin_bit_cast(bf16x8, r);
}
DI void glds16(__amdgpu_buffer_rsrc_t rs, lds_char* dst, unsigned voff, unsigned soff) {
  unsigned m = (unsigned)(size_t)dst;
  asm volatile("s_mov_b32 m0, %0\n\ts_nop 0\n\tbuffer_load_dwordx4 %1, %2, %3 offen" GPOL " lds"
               :: "s"(m), "v"(voff), "s"(rs), "s"(soff) : "memory");
}
DI void touch(__amdgpu_buffer_rsrc_t rs, unsigned voff, unsigned soff) {
  asm volatile("buffer_load_dword v250, %0, %1, %2 offen" TPOL :: "v"(voff), "s"(rs), "s"(soff) : "memory", "v250");
}
template <int IMM>
DI void stouch(__amdgpu_buffer_rsrc_t rs, unsigned soff) {
  asm volatile("s_buffer_load_dword s99, %0, %1 offset:%2" :: "s"(rs), "s"(soff), "n"(IMM) : "memory", "s99");
}
DI float xhalf_max(float v) {
  auto rr = __builtin_amdgcn_permlane32_swap(__float_as_uint(v), __float_as_uint(v), false, false);
  return fmaxf(__uint_as_float(rr[0]), __uint_as_float(rr[1]));
}
DI float max16(float m, const f32x16& a) {
#pragma unroll
  for (int i = 0; i < 16; i += 2) m = fmaxf(fmaxf(m, a[i]), a[i + 1]);
  return m;
}

struct Att { f32x16 o; f32x4 l; };

DI f32x16 qk_block(lds_char* kp, bf16x8 q0, bf16x8 q1, const f32x16& c) {
  bf16x8 k0 = lds_b128(kp), k1 = lds_b128(kp + 1024);
  f32x16 z = MFMA32(k0, q0, c);
  return MFMA32(k1, q1, z);
}
DI unsigned pexp(float a, float b) { return pk2(__builtin_amdgcn_exp2f(a), __builtin_amdgcn_exp2f(b)); }
DI void pv_block(lds_char* kp, const f32x16& sc, bf16x8 ones, Att& a) {
  u32x4 p0, p1;
#pragma unroll
  for (int i = 0; i < 4; ++i) { p0[i] = pexp(sc[2 * i], sc[2 * i + 1]); p1[i] = pexp(sc[8 + 2 * i], sc[9 + 2 * i]); }
  bf16x8 v0 = lds_b128(kp + 2048), v1 = lds_b128(kp + 3072);
  a.o = MFMA32(v0, __builtin_bit_cast(bf16x8, p0), a.o);
  a.l = MFMA16(ones, __builtin_bit_cast(bf16x8, p0), a.l);
  a.o = MFMA32(v1, __builtin_bit_cast(bf16x8, p1), a.o);
  a.l = MFMA16(ones, __builtin_bit_cast(bf16x8, p1), a.l);
}
DI void att_step(const f32x16& s, f32x16& sn, bf16x8 (&kf)[2], bf16x8 (&vf)[2], lds_char* p1, lds_char* p2, bf16x8 q0, bf16x8 q1,
                 const f32x16& negm, bf16x8 ones, Att& a) {
  u32x4 pa, pb;
  SB0();
  f32x16 z = MFMA32(kf[0], q0, negm);
  SB0();
  pa[0] = pexp(s[0], s[1]); pa[1] = pexp(s[2], s[3]);
  SB0();
  sn = MFMA32(kf[1], q1, z);
  kf[0] = lds_b128(p2); kf[1] = lds_b128(p2 + 1024);
  SB0();
  pa[2] = pexp(s[4], s[5]); pa[3] = pexp(s[6], s[7]);
  SB0();
  a.o = MFMA32(vf[0], __builtin_bit_cast(bf16x8, pa), a.o);
  SB0();
  pb[0] = pexp(s[8], s[9]); pb[1] = pexp(s[10], s[11]);
  SB0();
  a.l = MFMA16(ones, __builtin_bit_cast(bf16x8, pa), a.l);
  vf[0] = lds_b128(p1 + 2048);
  SB0();
  pb[2] = pexp(s[12], s[13]); pb[3] = pexp(s[14], s[15]);
  SB0();
  a.o = MFMA32(vf[1], __builtin_bit_cast(bf16x8, pb), a.o);
  a.l = MFMA16(ones, __builtin_bit_cast(bf16x8, pb), a.l);
  vf[1] = lds_b128(p1 + 3072);
  SB0();
}
DI void attn_exact(lds_char* kb, bf16x8 q0, bf16x8 q1, int r, int h, int qb, int jlo, bf16x8 ones, Att& a) {
  float m = -INFINITY;
  a.o = f32x16{}; a.l = f32x4{};
#pragma unroll 1
  for (int pass = 0; pass < 2; ++pass) {
    float mx = -INFINITY;
#pragma unroll 1
    for (int j = jlo; j <= qb + 8; ++j) {
      lds_char* kp = kb + (((unsigned)(KVPOS >> (4 * j)) & 15) << 13);
      f32x16 s = qk_block(kp, q0, q1, f32x16{});
#pragma unroll
      for (int i = 0; i < 16; ++i) {
        const bool above = (i & 3) + 8 * (i >> 2) + 4 * h > r;
        if ((j == qb && !above) || (j == qb + 8 && above)) s[i] = -INFINITY;
      }
      if (pass == 0) mx = max16(mx, s);
      else {
#pragma unroll
        for (int i = 0; i < 16; ++i) s[i] -= m;
        pv_block(kp, s, ones, a);
      }
    }
    if (pass == 0) m = xhalf_max(mx);
  }
}

__global__ __launch_bounds__(512, 2) void k_fused(const __hip_bfloat16* __restrict__ x, const __hip_bfloat16* __restrict__ w,
                                                  const __hip_bfloat16* __restrict__ wo, __hip_bfloat16* __restrict__ out,
                                                  char* __restrict__ ws) {
  extern __shared__ __attribute__((aligned(16))) char smem_[];
  lds_char* smem = (lds_char*)smem_;
  const int tid = threadIdx.x, lane = tid & 63;
  const int wv = __builtin_amdgcn_readfirstlane(tid >> 6);
  const int b = blockIdx.x, xcd = b & 7, idx = b >> 3;
  const int rb = (xcd >> 2) * 8 + (idx & 7);
  const int hp = (xcd & 3) * 4 + (idx >> 3);
  const int rbo = xcd * 2 + (idx & 1);
  const int rot = (RA * (xcd & 3) + RB * (xcd >> 2)) & 15;
  const int cbo = idx >> 1;
  __amdgpu_buffer_rsrc_t rx = __builtin_amdgcn_make_buffer_rsrc((void*)x, 0, S_LEN * DM * 2, 0x00020000);
  __amdgpu_buffer_rsrc_t rw = __builtin_amdgcn_make_buffer_rsrc((void*)w, 0, DM * 3 * DM * 2, 0x00020000);
  __amdgpu_buffer_rsrc_t ro = __builtin_amdgcn_make_buffer_rsrc((void*)wo, 0, DM * DM * 2, 0x00020000);
  __amdgpu_buffer_rsrc_t rkv = __builtin_amdgcn_make_buffer_rsrc((void*)(ws + WS_KV), 0, 8u << 20, 0x00020000);
  __amdgpu_buffer_rsrc_t rof = __builtin_amdgcn_make_buffer_rsrc((void*)(ws + WS_OF), 0, 4u << 20, 0x00020000);
  u64 pc0; int clen;
  asm volatile("s_getpc_b64 %0\n.Lsw_pc0:\n\ts_mov_b32 %1, .Lsw_end-.Lsw_pc0" : "=s"(pc0), "=s"(clen));
  __amdgpu_buffer_rsrc_t rc = __builtin_amdgcn_make_buffer_rsrc((void*)pc0, 0, clen > 0 ? clen : 0, 0x00020000);
  __amdgpu_buffer_rsrc_t rsy = __builtin_amdgcn_make_buffer_rsrc((void*)ws, 0, WS_SYNC, 0x00020000);

  asm volatile("" : "+s"(out));
  const int lw = wv & 3;
  unsigned vx[4];
#pragma unroll
  for (int i = 0; i < 4; ++i) {
    int row = i * 8 + (lane >> 3);
    int f = (((row >> 4) & 1) << 2) | ((row >> 1) & 3);
    vx[i] = (unsigned)((rb * 128 + lw * 32 + row) * 2048 + (((lane & 7) ^ f) << 4));
  }
  const unsigned vwo = (unsigned)((lane >> 3) * 2048 + cbo * 128 + (((lane & 7) ^ (((lane >> 4) & 3) << 1)) << 4));

  const int me = rbo * 16 + cbo, mek = hp * 16 + rb;
  const unsigned e_off = wv >= 4 ? (lane < 16 ? WS_EPO + (rb * 16 + lane) * 8 : (lane < 18 && rb + lane < 31) ? WS_EPK + (mek + lane - 15) * 8 : OOB)
                                 : (lane == 0 ? WS_EPK + mek * 8 : lane == 1 ? WS_EPO + me * 8 : OOB);
  u32x2 want;
  bf16x8 q0, q1;
  u32x4 hold[16];

  if (wv >= 4) {
    unsigned vw[6];
#pragma unroll
    for (int i = 0; i < 6; ++i) {
      int pi = 6 * lw + i, seg = pi >> 3, k = (pi & 7) * 8 + (lane >> 3);
      int lc = (lane & 7) ^ ((((k >> 1) & 1) | (((k >> 3) & 1) << 1)) << 1);
      vw[i] = (unsigned)(k * 6144 + seg * 2048 + hp * 128 + lc * 16);
    }
    auto stage = [&](int t, int slot) {
      const int kt = (t + rot) & 15;
      lds_char* base = smem + slot * STAGE;
#pragma unroll
      for (int i = 0; i < 4; ++i) glds16(rx, base + (4 * lw + i) * 1024, vx[i], kt * 128);
#pragma unroll
      for (int i = 0; i < 6; ++i) glds16(rw, base + XB + (6 * lw + i) * 1024, vw[i], kt * 64 * 6144);
    };
    const int tg = lane >> 4, tl = lane & 15;
    const __amdgpu_buffer_rsrc_t rt_ = lw < 2 ? rw : rx;
    const unsigned tstep = lw < 2 ? 192 * 2048 : 128, hm = lw < 2 ? 3 : 0, tn = lw < 2 ? 12 : (XT ? 16 : 0);
    const unsigned tb = (lw < 2 ? (idx & 7) * 24 + lw * 12 : rb * 128 + (idx >> 3) * 32 + (lw - 2) * 16) * 2048 + (lw < 2 ? (hp & 12) * 128 : 0);
    const unsigned tv1 = lane < tn ? tb + tl * 2048 : OOB;
    auto warm = [&](int t0, int n) {
      const unsigned k = (t0 + tg + rot) & 15;
      touch(rt_, tg < n && tl < tn ? tb + tl * 2048 + k * tstep + ((hp + k) & hm) * 128 : OOB, 0);
    };
    const __amdgpu_buffer_rsrc_t rb_ = lw < 2 ? rx : rw;
    const unsigned bstep = lw < 2 ? 128 : 192 * 2048;
    const unsigned bx = (rb * 128 + lw * 64) * 2048, bw = ((idx & 7) * 24 + (lw - 2) * 12) * 2048 + (hp & 12) * 128;
    const unsigned bv = lw < 2 ? bx + lane * 2048 : (lane < 48 ? bw + (lane >> 2) * 2048 + (lane & 3) * 128 : OOB);
    auto warm1 = [&](int t) {
      const unsigned k = (t + rot) & 15;
      touch(rb_, (t < 16 && ((t ^ hp) & 3) == 0) ? bv : OOB, k * bstep);
    };
#pragma unroll
    for (int i = 0; i < 6; ++i) glds16(rw, smem + XB + (6 * lw + i) * 1024, vw[i], rot * 64 * 6144);
    stage(1, 1);
    warm(2, 4); warm(6, TD - 2);
    u32x2 ep = __builtin_amdgcn_raw_buffer_load_b64(rsy, e_off, 0, 16);
    WAIT_V(13); BAR();
    stage(2, 2);
    stage(3, 3);
    stouch<0>(rc, (idx * 8 + lw * 2) * 128); stouch<64>(rc, (idx * 8 + lw * 2) * 128);
    stouch<128>(rc, (idx * 8 + lw * 2) * 128); stouch<192>(rc, (idx * 8 + lw * 2) * 128);
    u32x4 wa[16];
    auto wo_in = [&](int u) {
      const u32x4 v = __builtin_amdgcn_raw_buffer_load_b128(ro, vwo, (((u >> 2) * 4 + lw) * 32 + (u & 3) * 8) * 2048, 0);
      if (u < 16) wa[u] = v; else hold[u - 16] = v;
    };
#pragma unroll
    for (int kt = 0; kt < 16; ++kt) {
      constexpr auto n = [](int k) { return k < 12 ? 11 : k == 12 ? WN0 : WN1; };
      const int cnt = kt == 0 ? 23 : kt == 1 ? 21 : kt == 2 ? 22 : 1 + n(kt - 2) + n(kt - 1);
      if (kt < 15) asm volatile("s_waitcnt vmcnt(%0)" :: "n"(cnt) : "memory");
      BAR();
      if (kt < 12) { stage(kt + 4, kt & 3); warm1(kt + 4 + TD); }
      if (kt >= 12 && kt < 15) {
#pragma unroll
        for (int u = kt == 12 ? 0 : kt == 13 ? WN0 : WN0 + WN1; u < (kt == 12 ? WN0 : kt == 13 ? WN0 + WN1 : 32); ++u) wo_in(u);
      }
      if (kt >= WO0 && kt < WO0 + 8) {
        const unsigned o = b * 8192 + lw * 2048 + (kt - WO0) * 256;
        stouch<0>(ro, o); stouch<64>(ro, o); stouch<128>(ro, o); stouch<192>(ro, o);
      }
    }
    WAIT_L0();
    asm volatile("" : "+v"(ep));
    const u64 w64 = (((u64)ep[1] << 32) | ep[0]) + 1;
    want = u32x2{(unsigned)w64, (unsigned)(w64 >> 32)};
    BAR();
    if (wv == 4)
      __builtin_amdgcn_raw_buffer_store_b64(want, rsy, (lane >> 1) == 8 && e_off != OOB ? WS_FLK + ((mek + lane - 15) * 2 + 17 - lane) * 128 : OOB, 0, 16);
    q0 = lds_b128(smem + L_Q1 + lw * 4096 + lane * 16);
    q1 = lds_b128(smem + L_Q1 + lw * 4096 + 1024 + lane * 16);
#pragma unroll
    for (int u = 0; u < 16; ++u)
      lds_st128(smem + ((u >> 2) * 4 + lw < 6 ? L_WA0 : L_WA1) + ((u >> 2) * 4 + lw) * 4096 + (u & 3) * 1024 + lane * 16, wa[u]);
  } else {
#pragma unroll
    for (int i = 0; i < 4; ++i) glds16(rx, smem + (4 * lw + i) * 1024, vx[i], rot * 128);
    const u32x2 ep = __builtin_amdgcn_raw_buffer_load_b64(rsy, e_off, 0, 16);
    const int rp = wv & 1, hh = wv >> 1;
    const int r = lane & 15, g = lane >> 4;
    const int arow = (r & 7) + 16 * (r >> 3);
    const unsigned aoff = (rp * 64 + arow) * 128 + ((g ^ (((r >> 3) << 2) | ((r >> 1) & 3))) << 4);
    const int q4 = (lane & 15) >> 2, p4 = lane & 3;
    const int f = (q4 >> 1) | ((g & 1) << 1);
    const unsigned boff0 = XB + (8 * g + q4) * 128 + (((hh * 4 + 0 + (p4 >> 1)) ^ (f << 1)) << 4) + 8 * (p4 & 1);
    const unsigned boff1 = XB + (8 * g + q4) * 128 + (((hh * 4 + 2 + (p4 >> 1)) ^ (f << 1)) << 4) + 8 * (p4 & 1);
    f32x4 acc[4][6];
#pragma unroll
    for (int m = 0; m < 4; ++m)
#pragma unroll
      for (int n = 0; n < 6; ++n) acc[m][n] = f32x4{0, 0, 0, 0};
    WAIT_V(0); WAIT_V0C(); BAR();
    const u64 w64 = (((u64)ep[1] << 32) | ep[0]) + 1;
    want = u32x2{(unsigned)w64, (unsigned)(w64 >> 32)};
    bf16x8 fa[2][4], fb[2][6];
    auto rd = [&](int set, lds_char* base, int s, int parts) {
      lds_char* b0 = base + boff0 + s * 4096; lds_char* b1 = base + boff1 + s * 4096;
      if (parts & 1) fb[set][0] = lds_tr2(b0, b0 + 512); else fb[set][2] = lds_tr2(b0 + 8192, b0 + 8192 + 512);
      fa[set][0] = lds_b128(base + (aoff ^ (s << 6)));
      fa[set][1] = lds_b128(base + (aoff ^ (s << 6)) + 1024);
      fa[set][2] = lds_b128(base + (aoff ^ (s << 6)) + 4096);
      fa[set][3] = lds_b128(base + (aoff ^ (s << 6)) + 5120);
      if (parts & 1) fb[set][1] = lds_tr2(b1, b1 + 512);
      if (parts == 3) fb[set][2] = lds_tr2(b0 + 8192, b0 + 8192 + 512);
      if (parts & 2) {
        fb[set][3] = lds_tr2(b1 + 8192, b1 + 8192 + 512);
        fb[set][4] = lds_tr2(b0 + 16384, b0 + 16384 + 512);
        fb[set][5] = lds_tr2(b1 + 16384, b1 + 16384 + 512);
      }
    };
    auto mm = [&](int set, int parts) {
#pragma unroll
      for (int n = (parts & 1) ? 0 : 2; n < ((parts & 2) ? 6 : 2); ++n)
#pragma unroll
        for (int m = 0; m < 4; ++m)
          acc[m][n] = n < 4 ? MFMA16(fb[set][n], fa[set][m], acc[m][n]) : MFMA16(fa[set][m], fb[set][n], acc[m][n]);
    };
    rd(0, smem, 0, 3);
    for (int kt = 0; kt < 12; ++kt) {
      lds_char* base = smem + (kt & 3) * STAGE;
      lds_char* nbase = smem + ((kt + 1) & 3) * STAGE;
      SB0();
      rd(1, base, 1, 3);
      mm(0, 3);
#pragma unroll
      for (int i = 0; i < 16; ++i) { SGB(0x8, 1); SGB(0x100, 1); }
      SGB(0x8, 8);
      SB0();
      WAIT_L0(); BAR();
      SB0();
      rd(0, nbase, 0, 3);
      mm(1, 3);
#pragma unroll
      for (int i = 0; i < 16; ++i) { SGB(0x8, 1); SGB(0x100, 1); }
      SGB(0x8, 8);
      SB0();
    }
#pragma unroll
    for (int kt = 12; kt < 16; ++kt) {
      lds_char* base = smem + (kt & 3) * STAGE;
      SB0();
      rd(1, base, 1, 2);
      mm(0, 2);
#pragma unroll
      for (int i = 0; i < 12; ++i) { SGB(0x8, 1); SGB(0x100, 1); }
      SGB(0x8, 4);
      SB0();
      WAIT_L0(); BAR();
      SB0();
      if (kt < 15) rd(0, base + STAGE, 0, 2); else rd(0, smem, 0, 1);
      mm(1, 2);
#pragma unroll
      for (int i = 0; i < 12; ++i) { SGB(0x8, 1); SGB(0x100, 1); }
      SGB(0x8, 4);
      SB0();
    }
    const float qs = 0.17677669529663687f * 1.4426950408889634f;
    const unsigned gofs = (g >> 1) * 1024 + (g & 1) * 512;
    const unsigned kvo = (unsigned)((hp * 16 + rb) * 32768 + wv * 8192);
    const unsigned ko = gofs + arow * 16, vo_ = 2048 + gofs + r * 16;
    const unsigned kog = rb < 15 ? ko : OOB, vog = rb < 15 ? vo_ : OOB;
    u32x4 kv[8];
    auto kv_out = [&](int u) {
      const int mp = u >> 2, e = u & 1;
      const f32x4 a = (u & 2) ? acc[2 * mp][4 + e] : acc[2 * mp + e][2], c = (u & 2) ? acc[2 * mp + 1][4 + e] : acc[2 * mp + e][3];
      kv[u] = u32x4{pk2(a[0], a[1]), pk2(a[2], a[3]), pk2(c[0], c[1]), pk2(c[2], c[3])};
      __builtin_amdgcn_raw_buffer_store_b128(kv[u], rkv, ((u & 2) ? vog : kog) + mp * 4096 + e * ((u & 2) ? 256 : 128), kvo, 16);
    };
    lds_char* dkv = smem + (3 + rp * 5) * 8192 + hh * 4096;
    auto kv_lds = [&](int u) {
      const int mp = u >> 2, e = u & 1;
      lds_st128(dkv + ((u & 2) ? vo_ : ko) + mp * 8192 + e * ((u & 2) ? 256 : 128), kv[u]);
    };
#pragma unroll
    for (int st = 0; st < 8; ++st) {
      SB0();
      if (st < 7) rd((st + 1) & 1, smem + ((st + 1) >> 1) * STAGE, (st + 1) & 1, 1);
      mm(st & 1, 1);
      if (st < 4) { kv_out(2 * st); kv_out(2 * st + 1); } else { kv_lds(2 * st - 8); kv_lds(2 * st - 7); }
#pragma unroll
      for (int i = 0; i < 8; ++i) { SGB(0x8, 1); SGB(0x100, 1); SGB(0x2, 2); }
      SB0();
    }
#pragma unroll
    for (int m = 0; m < 4; ++m) {
      const f32x4 a = acc[m][0] * qs, c = acc[m][1] * qs;
      u32x4 v = {pk2(a[0], a[1]), pk2(a[2], a[3]), pk2(c[0], c[1]), pk2(c[2], c[3])};
      lds_st128(smem + (rp ? L_Q1 + 8192 - (m >> 1) * 8192 : L_Q0 + (m >> 1) * 8192) + hh * 4096 + gofs + (arow + 8 * (m & 1)) * 16, v);
    }
    asm volatile("s_waitcnt vmcnt(0) lgkmcnt(0)" ::: "memory");
    BAR();
    q0 = lds_b128(smem + L_Q0 + wv * 4096 + lane * 16);
    q1 = lds_b128(smem + L_Q0 + wv * 4096 + 1024 + lane * 16);
  }

  const int ha = wv & 1, qb = wv < 4 ? wv >> 1 : 5 - (wv >> 1);
  const int r32 = lane & 31, h2 = lane >> 5;
  lds_char* kb = smem + ha * 4096 + lane * 16;
  const unsigned one2 = (((lane >> 4) ^ (lane >> 2)) & 1) ? 0u : 0x3f803f80u;
  const bf16x8 ones = __builtin_bit_cast(bf16x8, u32x4{one2, one2, one2, one2});
  Att at; at.o = f32x16{}; at.l = f32x4{};
  f32x16 negm, c;
#pragma unroll
  for (int i = 0; i < 16; ++i) c[i] = (i & 3) + 8 * (i >> 2) + 4 * h2 > r32 ? -INFINITY : 0.f;
  unsigned seq = ((unsigned)(KVPOS >> (32 + 4 * qb)) & 15) | ((unsigned)(KVPOS >> 32) << 4);
  int left = qb + 1;
  lds_char *p0, *p1;
  bf16x8 kf[2], vf[2];
  f32x16 sa, sb;
  auto first = [&]() {
    p0 = kb + ((seq & 15) << 13);
    p1 = kb + ((seq >> 4 & 15) << 13);
    seq >>= 8;
    SB0();
    const bf16x8 k0 = lds_b128(p0), k1 = lds_b128(p0 + 1024);
    kf[0] = lds_b128(p1); kf[1] = lds_b128(p1 + 1024);
    vf[0] = lds_b128(p0 + 2048); vf[1] = lds_b128(p0 + 3072);
    SB0();
    sa = MFMA32(k1, q1, MFMA32(k0, q0, c));
  };
  first();
  {
    const float m = xhalf_max(max16(-INFINITY, sa));
    const float ca = rb >= 1 ? -m : -INFINITY, cb = rb == 1 ? -m : -INFINITY;
#pragma unroll
    for (int i = 0; i < 16; ++i) { sa[i] -= m; negm[i] = -m; c[i] = (i & 3) + 8 * (i >> 2) + 4 * h2 > r32 ? ca : cb; }
  }
#pragma unroll 1
  for (int phase = 0;; phase = 1) {
#pragma unroll 1
    for (;;) {
      p0 = kb + ((seq & 15) << 13);
      att_step(sa, sb, kf, vf, p1, p0, q0, q1, negm, ones, at);
      if (--left == 0) break;
      p1 = kb + ((seq >> 4 & 15) << 13);
      seq >>= 8;
      att_step(sb, sa, kf, vf, p0, p1, q0, q1, negm, ones, at);
      if (--left == 0) break;
    }
    if (phase) break;
    const int prb = rb - 2 + (wv >> 1);
    if (wv < 4 && prb >= 0) {
      const unsigned w0 = __builtin_amdgcn_readlane(want[0], 0), w1 = __builtin_amdgcn_readlane(want[1], 0);
#pragma unroll 1
      for (unsigned spins = 0; spins < SPIN_CAP; ++spins) {
        FRESH();
        u32x2 f = __builtin_amdgcn_raw_buffer_load_b64(rsy, 0, WS_FLK + (mek * 2 + (wv >> 1)) * 128, 16);
        if (__builtin_amdgcn_readfirstlane(f[0]) == w0 && __builtin_amdgcn_readfirstlane(f[1]) == w1) break;
      }
      FRESH();
      u32x4 t[16];
#pragma unroll
      for (int i = 0; i < 16; ++i)
        t[i] = __builtin_amdgcn_raw_buffer_load_b128(rkv, lane * 16 + (i & 3) * 1024, (hp * 16 + prb) * 32768 + ha * 16384 + (i >> 2) * 4096, 16);
      const unsigned pos = (unsigned)(KVPOS >> (16 * (wv >> 1)));
#pragma unroll
      for (int i = 0; i < 16; ++i) lds_st128(kb + ((pos >> (i >> 2) * 4 & 15) << 13) + (i & 3) * 1024, t[i]);
    }
    const int j0 = rb >= 2 ? qb : 8 - 4 * rb;
    seq = (unsigned)(KVPOS >> (4 * j0));
    left = rb ? 8 - j0 : 1;
    WAIT_L0();
    BAR();
    first();
  }
  {
    float z = 0.f;
#pragma unroll
    for (int i = 0; i < 16; ++i) z = __builtin_fmaf(at.o[i], 0.f, z);
    if (__builtin_expect(__any(!(at.l[0] < 1e30f) || z != 0.f), 0))
      attn_exact(kb, q0, q1, r32, h2, qb, rb >= 2 ? qb : 8 - rb * 4, ones, at);
    const float inv = __builtin_amdgcn_rcpf(at.l[0]);
#pragma unroll
    for (int i = 0; i < 16; ++i) at.o[i] *= inv;
    const unsigned vo = (r32 >> 4) * 1024 + h2 * 256 + (r32 & 15) * 16;
    const unsigned so = (unsigned)(((rb * 4 + qb) * 32 + hp * 2 + ha) * 2048);
    __builtin_amdgcn_raw_buffer_store_b128(__builtin_bit_cast(u32x4, pack8(at.o, 0)), rof, vo, so, 16);
    __builtin_amdgcn_raw_buffer_store_b128(__builtin_bit_cast(u32x4, pack8(at.o, 1)), rof, vo + 512, so, 16);
    WAIT_V(0);
  }
  BAR();

  const int qo = wv & 3, kh = wv >> 2;
  if (kh) {
    if (wv == 4) __builtin_amdgcn_raw_buffer_store_b64(want, rsy, lane < 16 ? WS_FLO + ((rb * 16 + lane) * 16 + hp) * 128 : OOB, 0, 16);
#pragma unroll
    for (int u = 0; u < 16; ++u) lds_st128(smem + L_WB + ((u >> 2) * 4 + qo) * 4096 + (u & 3) * 1024 + lane * 16, hold[u]);
    WAIT_L0();
  } else if (wv == 0) {
    const unsigned w0 = __builtin_amdgcn_readlane(want[0], 1), w1 = __builtin_amdgcn_readlane(want[1], 1);
#pragma unroll 1
    for (unsigned spins = 0; spins < SPIN_CAP; ++spins) {
      FRESH();
      u32x2 f = __builtin_amdgcn_raw_buffer_load_b64(rsy, (lane & 15) * 128, WS_FLO + me * 2048, 16);
      if (__all(f[0] == w0 && f[1] == w1)) break;
    }
  }
  BAR();
  u32x4 of[32];
  const unsigned ob = (unsigned)(((rbo * 4 + qo) * 32 + kh * 16) * 2048), ov0 = lane * 16 + kh * 1024, ov1 = ov0 ^ 1024;
  auto o_in = [&](int i) {
    of[2 * i] = __builtin_amdgcn_raw_buffer_load_b128(rof, ov0 + (i & 1) * 2048, ob + (i >> 1) * 4096, 16);
    of[2 * i + 1] = __builtin_amdgcn_raw_buffer_load_b128(rof, ov1 + (i & 1) * 2048, ob + (i >> 1) * 4096, 16);
  };
#pragma unroll
  for (int i = 0; i < OD; ++i) o_in(i);
  f32x4 oc[2][4];
#pragma unroll
  for (int i = 0; i < 8; ++i) oc[i >> 2][i & 3] = f32x4{0, 0, 0, 0};
  {
    const int kg = lane >> 4, q4 = (lane & 15) >> 2, p4 = lane & 3;
    const int swz = 2 * (kg & 1) + (q4 >> 1);
    const unsigned rowo = (16 * (kg >> 1) + 4 * (kg & 1) + q4) * 128 + p4 * 8;
    lds_char* wb0 = smem + (kh ? L_WB : L_WA0) + rowo;
    lds_char* wb1 = smem + (kh ? L_WB : L_WA1) + rowo;
    bf16x8 wf[2][4];
    auto rdw = [&](int set, int i) {
      lds_char* base = (i < 6 ? wb0 : wb1) + i * 4096;
#pragma unroll
      for (int c = 0; c < 4; ++c) {
        lds_char* p = base + ((c ^ swz) << 5);
        wf[set][c] = lds_tr2(p, p + 1024);
      }
    };
    rdw(0, 0);
#pragma unroll
    for (int i = 0; i < 16; ++i) {
      SB0();
      if (i + OD < 16) o_in(i + OD);
      SB0();
      if (i < 15) rdw((i + 1) & 1, i + 1);
#pragma unroll
      for (int c = 0; c < 4; ++c)
#pragma unroll
        for (int q = 0; q < 2; ++q) oc[q][c] = MFMA16(wf[i & 1][c], __builtin_bit_cast(bf16x8, of[2 * i + q]), oc[q][c]);
#pragma unroll
      for (int k = 0; k < 8; ++k) { SGB(0x8, 1); SGB(0x100, 1); }
      SB0();
    }
  }
  {
    lds_char* xp = smem + (kh ? L_XC1 : L_XC0) + qo * 4096 + lane * 16;
    lds_char* xq = smem + (kh ? L_XC0 : L_XC1) + qo * 4096 + lane * 16;
#pragma unroll
    for (int c = 0; c < 4; ++c) lds_st128(xp + c * 1024, __builtin_bit_cast(u32x4, oc[1][c]));
    WAIT_L0();
    BAR();
    f32x4 fin[4];
#pragma unroll
    for (int c = 0; c < 4; ++c) fin[c] = oc[0][c] + *(__attribute__((address_space(3))) f32x4*)(xq + c * 1024);
    const int g4 = lane >> 4;
    char* dst = (char*)out + (size_t)(rbo * 128 + qo * 32 + kh * 16 + (lane & 15)) * 2048 + cbo * 128 + (g4 & 1) * 32 + (g4 >> 1) * 16;
#pragma unroll
    for (int u = 0; u < 2; ++u) {
      unsigned a0 = pk2(fin[2 * u][0], fin[2 * u][1]), a1 = pk2(fin[2 * u][2], fin[2 * u][3]);
      unsigned b0 = pk2(fin[2 * u + 1][0], fin[2 * u + 1][1]), b1 = pk2(fin[2 * u + 1][2], fin[2 * u + 1][3]);
      auto s0 = __builtin_amdgcn_permlane16_swap(a0, b0, false, false);
      auto s1 = __builtin_amdgcn_permlane16_swap(a1, b1, false, false);
      u32x4 v = {s0[0], s1[0], s0[1], s1[1]};
      *(u32x4*)(dst + u * 64) = v;
    }
    if (wv == 0) __builtin_amdgcn_raw_buffer_store_b64(want, rsy, e_off, 0, 16);
  }
  asm volatile(".Lsw_end:");
}

extern "C" void kernel_launch(void* const* d_in, const int* in_sizes, int n_in, void* d_out, int out_size,
                              void* d_ws, size_t ws_size, hipStream_t stream) {
  (void)hipFuncSetAttribute((const void*)k_fused, hipFuncAttributeMaxDynamicSharedMemorySize, LDS_BYTES);
  k_fused<<<256, 512, LDS_BYTES, stream>>>((const __hip_bfloat16*)d_in[0], (const __hip_bfloat16*)d_in[1],
                                           (const __hip_bfloat16*)d_in[2], (__hip_bfloat16*)d_out, (char*)d_ws);
}
```

```cpp
#include <hip/hip_runtime.h>
#include <hip/hip_bf16.h>
#include <stdint.h>

typedef __bf16 bf16x8 __attribute__((ext_vector_type(8)));
typedef __bf16 bf16x2 __attribute__((ext_vector_type(2)));
typedef short s16x4 __attribute__((ext_vector_type(4)));
typedef float f32x16 __attribute__((ext_vector_type(16)));
typedef float f32x4 __attribute__((ext_vector_type(4)));
typedef float f32x2 __attribute__((ext_vector_type(2)));
typedef unsigned u32x4 __attribute__((ext_vector_type(4)));
typedef unsigned u32x2 __attribute__((ext_vector_type(2)));
typedef __attribute__((address_space(3))) char lds_char;
typedef unsigned long long u64;
typedef __attribute__((address_space(1))) u64 gu64;

#define DI __device__ __forceinline__
#define MFMA32(a, b, c) __builtin_amdgcn_mfma_f32_32x32x16_bf16((a), (b), (c), 0, 0, 0)
#define MFMA16(a, b, c) __builtin_amdgcn_mfma_f32_16x16x32_bf16((a), (b), (c), 0, 0, 0)
#define WAIT_V(n) asm volatile("s_waitcnt vmcnt(" #n ")" ::: "memory")
#define WAIT_L0() __builtin_amdgcn_s_waitcnt(0xC07F)
#define WAIT_V0C() __builtin_amdgcn_s_waitcnt(0x0F70)
#define FRESH() asm volatile("" ::: "memory")
#define BAR() __builtin_amdgcn_s_barrier()
#define SB0() __builtin_amdgcn_sched_barrier(0)
#define SGB(m, n) __builtin_amdgcn_sched_group_barrier((m), (n), 0)
#define RLX_AGENT __ATOMIC_RELAXED, __HIP_MEMORY_SCOPE_AGENT

constexpr int S_LEN = 2048, DM = 1024;

constexpr size_t WS_EPK = 0;
constexpr size_t WS_EPO = 2048;
constexpr size_t WS_FLK = 65536;
constexpr size_t WS_FLO = 131072;
constexpr size_t WS_SYNC = WS_FLO + 256 * 16 * 128;
constexpr size_t WS_KV = 1u << 20;
constexpr size_t WS_OF = WS_KV + (8u << 20);
constexpr size_t WS_ST = WS_OF + (4u << 20);
constexpr unsigned OOB = 0x80000000u;

constexpr int STAGE = 40960, XB = 16384;
constexpr u64 KVPOS = 0x9843765ED210ull;
constexpr int L_Q0 = 13 * 8192, L_Q1 = 18 * 8192;
constexpr int L_WA0 = 10 * 8192, L_WA1 = 15 * 8192 - 6 * 4096;
constexpr int L_WB = 0;
constexpr int L_XC0 = 8 * 8192, L_XC1 = 13 * 8192;
constexpr int LDS_BYTES = 163840;
constexpr unsigned SPIN_CAP = 1u << 16;

#define GPOL ""
#define TPOL ""
#define RA 0
#define RB 0
#define PRO 0
#define TD 2
#define XT 1
#define WO0 0
#define WN2 8
#define WN0 12
#define WN1 12
#define WT0 99
#define PRI 0
#define SLP 4
#define P1 4
#define WOS 2
#define RBX 2
#define NOWO 0
#define QS 4
#define SPS 3
#define HALF 0
#define SST 4
#define OD 3
DI unsigned pk2(float a, float b) {
  f32x2 v = {a, b};
  bf16x2 r = __builtin_convertvector(v, bf16x2);
  return __builtin_bit_cast(unsigned, r);
}
DI bf16x8 pack8(const f32x16& a, int s) {
  u32x4 r;
  r[0] = pk2(a[8 * s + 0], a[8 * s + 1]);
  r[1] = pk2(a[8 * s + 2], a[8 * s + 3]);
  r[2] = pk2(a[8 * s + 4], a[8 * s + 5]);
  r[3] = pk2(a[8 * s + 6], a[8 * s + 7]);
  return __builtin_bit_cast(bf16x8, r);
}
DI bf16x8 lds_b128(lds_char* p) { return *(__attribute__((address_space(3))) bf16x8*)p; }
DI void lds_st128(lds_char* p, u32x4 v) { *(__attribute__((address_space(3))) u32x4*)p = v; }
DI bf16x8 lds_tr2(lds_char* p0, lds_char* p1) {
  s16x4 a = __builtin_amdgcn_ds_read_tr16_b64_v4i16((__attribute__((address_space(3))) s16x4*)p0);
  s16x4 b = __builtin_amdgcn_ds_read_tr16_b64_v4i16((__attribute__((address_space(3))) s16x4*)p1);
  u32x2 ua = __builtin_bit_cast(u32x2, a), ub = __builtin_bit_cast(u32x2, b);
  u32x4 r = {ua[0], ua[1], ub[0], ub[1]};
  return __builtin_bit_cast(bf16x8, r);
}
DI void glds16(__amdgpu_buffer_rsrc_t rs, lds_char* dst, unsigned voff, unsigned soff) {
  unsigned m = (unsigned)(size_t)dst;
  asm volatile("s_mov_b32 m0, %0\n\ts_nop 0\n\tbuffer_load_dwordx4 %1, %2, %3 offen" GPOL " lds"
               :: "s"(m), "v"(voff), "s"(rs), "s"(soff) : "memory");
}
DI void touch(__amdgpu_buffer_rsrc_t rs, unsigned voff, unsigned soff) {
  asm volatile("buffer_load_dword v255, %0, %1, %2 offen" TPOL :: "v"(voff), "s"(rs), "s"(soff) : "memory", "v255");
}
#define LD4(a, b, c, d, o) "buffer_load_dwordx4 %" #a ", %16, %17, %" #o " offen sc1\n\tbuffer_load_dwordx4 %" #b ", %16, %17, %" #o " offen offset:1024 sc1\n\t" \
                           "buffer_load_dwordx4 %" #c ", %16, %17, %" #o " offen offset:2048 sc1\n\tbuffer_load_dwordx4 %" #d ", %16, %17, %" #o " offen offset:3072 sc1\n\t"
#define ST1(a, p, o, n) "s_waitcnt vmcnt(" #n ")\n\tds_write_b128 %" #p ", %" #a " offset:" #o "\n\t"
DI void fetch16(const u32x4 (&t)[16], __amdgpu_buffer_rsrc_t rs, unsigned voff, unsigned so, lds_char* d0, lds_char* d1, lds_char* d2, lds_char* d3) {
  asm volatile(LD4(0, 1, 2, 3, 18) LD4(4, 5, 6, 7, 19) LD4(8, 9, 10, 11, 20) LD4(12, 13, 14, 15, 21)
               ST1(0, 22, 0, 15) ST1(1, 22, 1024, 14) ST1(2, 22, 2048, 13) ST1(3, 22, 3072, 12)
               ST1(4, 23, 0, 11) ST1(5, 23, 1024, 10) ST1(6, 23, 2048, 9) ST1(7, 23, 3072, 8)
               ST1(8, 24, 0, 7) ST1(9, 24, 1024, 6) ST1(10, 24, 2048, 5) ST1(11, 24, 3072, 4)
               ST1(12, 25, 0, 3) ST1(13, 25, 1024, 2) ST1(14, 25, 2048, 1) ST1(15, 25, 3072, 0) "s_waitcnt lgkmcnt(0)"
               :: "v"(t[0]), "v"(t[1]), "v"(t[2]), "v"(t[3]), "v"(t[4]), "v"(t[5]), "v"(t[6]), "v"(t[7]),
                  "v"(t[8]), "v"(t[9]), "v"(t[10]), "v"(t[11]), "v"(t[12]), "v"(t[13]), "v"(t[14]), "v"(t[15]),
                  "v"(voff), "s"(rs), "s"(so), "s"(so + 4096), "s"(so + 8192), "s"(so + 12288), "v"(d0), "v"(d1), "v"(d2), "v"(d3) : "memory");
}
template <int IMM>
DI void stouch(__amdgpu_buffer_rsrc_t rs, unsigned soff) {
  asm volatile("s_buffer_load_dword s101, %0, %1 offset:%2" :: "s"(rs), "s"(soff), "n"(IMM) : "memory", "s101");
}
DI int lane_id() {
  int l;
  asm volatile("v_mbcnt_lo_u32_b32 %0, -1, 0\n\tv_mbcnt_hi_u32_b32 %0, -1, %0" : "=v"(l));
  return l;
}
DI f32x16 tri(int l, float a, float b) {
  const int t = (int)((0xfffffffeu << (l & 31)) >> (l >> 5 << 2));
  f32x16 c;
#pragma unroll
  for (int i = 0; i < 16; ++i) {
    const unsigned m = __builtin_amdgcn_sbfe(t, (i & 3) + 8 * (i >> 2), 1);
    c[i] = __uint_as_float((m & __float_as_uint(a)) | (~m & __float_as_uint(b)));
  }
  return c;
}
DI float xhalf_max(float v) {
  auto rr = __builtin_amdgcn_permlane32_swap(__float_as_uint(v), __float_as_uint(v), false, false);
  return fmaxf(__uint_as_float(rr[0]), __uint_as_float(rr[1]));
}
DI float max16(float m, const f32x16& a) {
#pragma unroll
  for (int i = 0; i < 16; i += 2) m = fmaxf(fmaxf(m, a[i]), a[i + 1]);
  return m;
}

struct Att { f32x16 o; f32x4 l; };

DI f32x16 qk_block(lds_char* kp, bf16x8 q0, bf16x8 q1, const f32x16& c) {
  bf16x8 k0 = lds_b128(kp), k1 = lds_b128(kp + 1024);
  f32x16 z = MFMA32(k0, q0, c);
  return MFMA32(k1, q1, z);
}
DI unsigned pexp(float a, float b) { return pk2(__builtin_amdgcn_exp2f(a), __builtin_amdgcn_exp2f(b)); }
DI void pv_block(lds_char* kp, const f32x16& sc, bf16x8 ones, Att& a) {
  u32x4 p0, p1;
#pragma unroll
  for (int i = 0; i < 4; ++i) { p0[i] = pexp(sc[2 * i], sc[2 * i + 1]); p1[i] = pexp(sc[8 + 2 * i], sc[9 + 2 * i]); }
  bf16x8 v0 = lds_b128(kp + 2048), v1 = lds_b128(kp + 3072);
  a.o = MFMA32(v0, __builtin_bit_cast(bf16x8, p0), a.o);
  a.l = MFMA16(ones, __builtin_bit_cast(bf16x8, p0), a.l);
  a.o = MFMA32(v1, __builtin_bit_cast(bf16x8, p1), a.o);
  a.l = MFMA16(ones, __builtin_bit_cast(bf16x8, p1), a.l);
}
DI void att_step(const f32x16& s, f32x16& sn, bf16x8 (&kf)[2], bf16x8 (&vf)[2], lds_char* p1, lds_char* p2, bf16x8 q0, bf16x8 q1,
                 const f32x16& negm, bf16x8 ones, Att& a) {
  u32x4 pa, pb;
  SB0();
  f32x16 z = MFMA32(kf[0], q0, negm);
  SB0();
  pa[0] = pexp(s[0], s[1]); pa[1] = pexp(s[2], s[3]);
  SB0();
  sn = MFMA32(kf[1], q1, z);
  kf[0] = lds_b128(p2); kf[1] = lds_b128(p2 + 1024);
  SB0();
  pa[2] = pexp(s[4], s[5]); pa[3] = pexp(s[6], s[7]);
  SB0();
  a.o = MFMA32(vf[0], __builtin_bit_cast(bf16x8, pa), a.o);
  SB0();
  pb[0] = pexp(s[8], s[9]); pb[1] = pexp(s[10], s[11]);
  SB0();
  a.l = MFMA16(ones, __builtin_bit_cast(bf16x8, pa), a.l);
  vf[0] = lds_b128(p1 + 2048);
  SB0();
  pb[2] = pexp(s[12], s[13]); pb[3] = pexp(s[14], s[15]);
  SB0();
  a.o = MFMA32(vf[1], __builtin_bit_cast(bf16x8, pb), a.o);
  a.l = MFMA16(ones, __builtin_bit_cast(bf16x8, pb), a.l);
  vf[1] = lds_b128(p1 + 3072);
  SB0();
}
DI void attn_exact(lds_char* kb, bf16x8 q0, bf16x8 q1, int r, int h, int qb, int jlo, bf16x8 ones, Att& a) {
  float m = -INFINITY;
  a.o = f32x16{}; a.l = f32x4{};
#pragma unroll 1
  for (int pass = 0; pass < 2; ++pass) {
    float mx = -INFINITY;
#pragma unroll 1
    for (int j = jlo; j <= qb + 8; ++j) {
      lds_char* kp = kb + (((unsigned)(KVPOS >> (4 * j)) & 15) << 13);
      f32x16 s = qk_block(kp, q0, q1, f32x16{});
#pragma unroll
      for (int i = 0; i < 16; ++i) {
        const bool above = (i & 3) + 8 * (i >> 2) + 4 * h > r;
        if ((j == qb && !above) || (j == qb + 8 && above)) s[i] = -INFINITY;
      }
      if (pass == 0) mx = max16(mx, s);
      else {
#pragma unroll
        for (int i = 0; i < 16; ++i) s[i] -= m;
        pv_block(kp, s, ones, a);
      }
    }
    if (pass == 0) m = xhalf_max(mx);
  }
}

__global__ __launch_bounds__(512, 2) void k_fused(const __hip_bfloat16* __restrict__, const __hip_bfloat16* __restrict__,
                                                  const __hip_bfloat16* __restrict__, __hip_bfloat16* __restrict__, char* __restrict__) {
  extern __shared__ __attribute__((aligned(16))) char smem_[];
  lds_char* smem = (lds_char*)smem_;
  u64 a0, a1, a2, a3, a4, x, w, wo, out, ws;
  asm volatile("s_load_dwordx2 %0, %5, 0x0\n\ts_load_dwordx2 %1, %5, 0x8\n\ts_load_dwordx2 %2, %5, 0x10\n\t"
               "s_load_dwordx2 %3, %5, 0x18\n\ts_load_dwordx2 %4, %5, 0x20"
               : "=&s"(a0), "=&s"(a1), "=&s"(a2), "=&s"(a3), "=&s"(a4) : "s"(__builtin_amdgcn_kernarg_segment_ptr()));
  const int tid = threadIdx.x, lane = tid & 63;
  const int wv = __builtin_amdgcn_readfirstlane(tid >> 6);
  const int b = blockIdx.x, xcd = b & 7, idx = b >> 3;
  const int rb = (xcd >> 2) * 8 + (idx & 7);
  const int hp = (xcd & 3) * 4 + (idx >> 3);
  const int rbo = (xcd >> (RBX - 1) << RBX) + (idx & ((1 << RBX) - 1));
  const int cbo = ((xcd & ((1 << (RBX - 1)) - 1)) << (5 - RBX)) + (idx >> RBX);
  u64 pc0; int clen;
  asm volatile("s_getpc_b64 %0\n.Lsw_pc0:\n\ts_mov_b32 %1, .Lsw_end-.Lsw_pc0" : "=s"(pc0), "=s"(clen));
  __amdgpu_buffer_rsrc_t rc = __builtin_amdgcn_make_buffer_rsrc((void*)pc0, 0, clen > 0 ? clen : 0, 0x00020000);

  touch(rc, (idx * 8 + wv) * 128 + lane * 0x100000, 0);
  SB0();
  const int lw = wv & 3;
  unsigned tx = (rb * 128 + (idx >> 3) * 32 + lane) * 2048 + (lw + 1) * 128;
  unsigned tw = ((lw + 1) * 192 + (idx & 7) * 24 + lane) * 2048 + (hp & 12) * 128 + ((hp + wv) & 3) * 128;
  asm volatile("" : "+v"(tx), "+v"(tw));
  tx = lane < 32 ? tx : OOB; tw = lane < 24 ? tw : OOB;
  unsigned vx[4], vw[2];
#pragma unroll
  for (int i = 0; i < 4; ++i) {
    int row = i * 8 + (lane >> 3);
    int f = (((row >> 4) & 1) << 2) | ((row >> 1) & 3);
    vx[i] = (unsigned)((rb * 128 + lw * 32 + row) * 2048 + (((lane & 7) ^ f) << 4));
  }
#pragma unroll
  for (int i = 0; i < 2; ++i)
    vw[i] = (unsigned)((lw * 16 + i * 8 + (lane >> 3)) * 6144 + hp * 128 + (((lane & 7) ^ ((((lane >> 4) & 1) | (i << 1)) << 1)) << 4));
  asm volatile("" : "+v"(tx), "+v"(tw), "+v"(vx[0]), "+v"(vx[1]), "+v"(vx[2]), "+v"(vx[3]), "+v"(vw[0]), "+v"(vw[1]));
  asm volatile("s_waitcnt lgkmcnt(0)\n\ts_mov_b64 %0, %5\n\ts_mov_b64 %1, %6\n\ts_mov_b64 %2, %7\n\ts_mov_b64 %3, %8\n\ts_mov_b64 %4, %9"
               : "=&s"(x), "=&s"(w), "=&s"(wo), "=&s"(out), "=&s"(ws) : "s"(a0), "s"(a1), "s"(a2), "s"(a3), "s"(a4));
  __amdgpu_buffer_rsrc_t rx = __builtin_amdgcn_make_buffer_rsrc((void*)x, 0, S_LEN * DM * 2, 0x00020000);
  __amdgpu_buffer_rsrc_t rw = __builtin_amdgcn_make_buffer_rsrc((void*)w, 0, DM * 3 * DM * 2, 0x00020000);
  __amdgpu_buffer_rsrc_t ro = __builtin_amdgcn_make_buffer_rsrc((void*)wo, 0, DM * DM * 2, 0x00020000);
  __amdgpu_buffer_rsrc_t rkv = __builtin_amdgcn_make_buffer_rsrc((void*)(ws + WS_KV), 0, 8u << 20, 0x00020000);
  __amdgpu_buffer_rsrc_t rof = __builtin_amdgcn_make_buffer_rsrc((void*)(ws + WS_OF), 0, 4u << 20, 0x00020000);
  __amdgpu_buffer_rsrc_t rsy = __builtin_amdgcn_make_buffer_rsrc((void*)ws, 0, WS_SYNC, 0x00020000);
  auto stage = [&](int kt, int slot, int i0 = 0, int i1 = 10) {
    lds_char* base = smem + slot * STAGE;
#pragma unroll
    for (int i = i0; i < i1; ++i) {
      const int j = i - 4;
      if (i < 4) glds16(rx, base + (4 * lw + i) * 1024, vx[i], kt * 128);
      else glds16(rw, base + XB + (j >> 1) * 8192 + (2 * lw + (j & 1)) * 1024, vw[j & 1], kt * 64 * 6144 + (j >> 1) * 2048);
    }
  };
  if (wv < 4) stage(0, 0);
  else {
    if (lw & 1) __builtin_amdgcn_s_sleep(SLP);
    if (lw & 2) __builtin_amdgcn_s_sleep(2 * SLP);
    touch(rx, tx, 0); touch(rw, tw, 0);
    stage(1, 1, 0, P1);
  }
  int role = wv;
  asm volatile("" : "+s"(role));
  const unsigned vwo = (unsigned)((lane >> 3) * 2048 + cbo * 128 + (((lane & 7) ^ (((lane >> 4) & 3) << 1)) << 4));

  const int me = rbo * 16 + cbo, mek = hp * 16 + rb;
  const unsigned e_off = wv >= 4 ? (lane < 16 ? WS_EPO + (rb * 16 + lane) * 8 : (lane < 18 && rb + lane < 31) ? WS_EPK + (mek + lane - 15) * 8 : OOB)
                                 : (lane == 0 ? WS_EPK + mek * 8 : lane == 1 ? WS_EPO + me * 8 : OOB);
  u32x2 want;
  u32x4 wa[16], hold[16];
  const int ha = wv & 1, qb = wv < 4 ? wv >> 1 : 5 - (wv >> 1);
  lds_char *kb, *qp;
  bf16x8 ones;
  Att at;
  f32x16 c;
  unsigned fk;
  auto setup = [&](int l) {
    kb = smem + ha * 4096 + l * 16;
    qp = smem + (wv < 4 ? L_Q0 : L_Q1) + lw * 4096 + l * 16;
    const unsigned one2 = (((l >> 4) ^ (l >> 2)) & 1) ? 0u : 0x3f803f80u;
    ones = __builtin_bit_cast(bf16x8, u32x4{one2, one2, one2, one2});
    at.o = f32x16{}; at.l = f32x4{};
    c = tri(l, -INFINITY, 0.f);
    fk = (unsigned)(l - 16) < (unsigned)(rb < 14 ? 2 : 15 - rb) ? WS_FLK + ((mek + l - 15) * 2 + 17 - l) * 128 : OOB;
  };
  auto pin = [&]() { asm volatile("" : "+v"(c), "+v"(ones), "+v"(fk), "+v"(at.o), "+v"(at.l), "+v"(qp), "+v"(kb), "+v"(want)); };

  if (role >= 4) {
    BAR();
    stage(1, 1, P1);
    touch(rx, lw < TD - 1 ? tx : OOB, 512); touch(rw, lw < TD - 1 ? tw : OOB, 4 * 192 * 2048);
    u32x2 ep = __builtin_amdgcn_raw_buffer_load_b64(rsy, e_off, 0, 16);
    stage(2, 2);
    const __amdgpu_buffer_rsrc_t rb_ = lw < 2 ? rx : rw;
    const unsigned bstep = lw < 2 ? 128 : 192 * 2048;
    const unsigned bx = (rb * 128 + lw * 64) * 2048, bw = ((idx & 7) * 24 + (lw - 2) * 12) * 2048 + (hp & 12) * 128;
    const unsigned bv = lw < 2 ? bx + lane * 2048 : (lane < 48 ? bw + (lane >> 2) * 2048 + (lane & 3) * 128 : OOB);
    auto warm1 = [&](int t) { touch(rb_, (t < 16 && ((t ^ hp) & 3) == 0) ? bv : OOB, (t & 15) * bstep); };
    auto wo_in = [&](int u) {
      const u32x4 v = __builtin_amdgcn_raw_buffer_load_b128(ro, vwo | NOWO, (((u >> 2) * 4 + lw) * 32 + (u & 3) * 8) * 2048, 0);
      if (u < 16) wa[u] = v; else hold[u - 16] = v;
    };
    constexpr auto wc = [](int k) { return k <= 12 ? 0 : k == 13 ? WN0 : k == 14 ? WN0 + WN1 : 32; };
#pragma unroll
    for (int kt = 0; kt < 15; ++kt) {
      constexpr auto n = [wc](int k) { return (k < 12 ? 11 : 0) + (k >= WT0 && k < WT0 + 4) + wc(k + 1) - wc(k); };
      const int cnt = kt == 0 ? 13 : kt == 1 ? 10 + n(0) : kt == 2 ? n(0) + n(1) : kt == 12 ? n(10) - 10 + n(11) : kt == 13 ? n(11) - 10 + n(12)
                    : n(kt - 3) - 10 + n(kt - 2) + n(kt - 1);
      if (kt < 14) asm volatile("s_waitcnt vmcnt(%0)" :: "n"(cnt) : "memory");
      BAR();
      if (kt == 0) stage(3, 3);
      if (kt < 12) { stage(kt + 4, kt & 3); warm1(kt + 4 + TD); }
      if (kt >= WT0 && kt < WT0 + 4) touch(ro, (((kt + TD) ^ hp) & 3) == 0 ? ((idx * 4 + lw) * 8 + (lane >> 3)) * 2048 + ((cbo & 8) + (lane & 7)) * 128 : OOB, 0);
#pragma unroll
      for (int u = wc(kt); u < wc(kt + 1); ++u) wo_in(u);
      if (kt >= WO0 && kt < WO0 + 8 / WOS) {
#pragma unroll
        for (int j = 0; j < WOS; ++j) {
          const unsigned o = b * 8192 + lw * 2048 + ((kt - WO0) * WOS + j) * 256;
          stouch<0>(ro, o); stouch<64>(ro, o); stouch<128>(ro, o); stouch<192>(ro, o);
        }
      }
    }
    asm volatile("" : "+v"(ep));
    const u64 w64 = (((u64)ep[1] << 32) | ep[0]) + 1;
    want = u32x2{(unsigned)w64, (unsigned)(w64 >> 32)};
    setup(lane_id()); pin();
  } else {
    u32x2 ep = __builtin_amdgcn_raw_buffer_load_b64(rsy, e_off, 0, 16);
    const int rp = wv & 1, hh = wv >> 1;
    const int r = lane & 15, g = lane >> 4;
    const int arow = (r & 7) + 16 * (r >> 3);
    const unsigned aoff = (rp * 64 + arow) * 128 + ((g ^ (((r >> 3) << 2) | ((r >> 1) & 3))) << 4);
    const int q4 = (lane & 15) >> 2, p4 = lane & 3;
    const int f = (q4 >> 1) | ((g & 1) << 1);
    const unsigned boff0 = XB + (8 * g + q4) * 128 + (((hh * 4 + 0 + (p4 >> 1)) ^ (f << 1)) << 4) + 8 * (p4 & 1);
    const unsigned boff1 = XB + (8 * g + q4) * 128 + (((hh * 4 + 2 + (p4 >> 1)) ^ (f << 1)) << 4) + 8 * (p4 & 1);
    f32x4 acc[4][6];
#pragma unroll
    for (int m = 0; m < 4; ++m)
#pragma unroll
      for (int n = 0; n < 6; ++n) acc[m][n] = f32x4{0, 0, 0, 0};
    const float qs = 0.17677669529663687f * 1.4426950408889634f;
    const unsigned gofs = (g >> 1) * 1024 + (g & 1) * 512;
    const unsigned kvo = (unsigned)((hp * 16 + rb) * 32768 + wv * 8192);
    const unsigned ko = gofs + arow * 16, vo_ = 2048 + gofs + r * 16;
    unsigned kog = rb < 15 ? ko : OOB, vog = rb < 15 && HALF < 1 ? vo_ : OOB;
    if (HALF > 1) kog = OOB;
    lds_char* dk = smem + (3 + rp * 5) * 8192 + hh * 4096 + ko;
    lds_char* dv = smem + (3 + rp * 5) * 8192 + hh * 4096 + vo_;
    lds_char* dq0 = smem + (rp ? L_Q1 + 8192 : L_Q0) + hh * 4096 + ko;
    lds_char* dq1 = smem + (rp ? L_Q1 : L_Q0 + 8192) + hh * 4096 + ko;
    asm volatile("" : "+v"(kog), "+v"(vog), "+v"(dk), "+v"(dv), "+v"(dq0), "+v"(dq1));
    WAIT_V(1); BAR();
    bf16x8 fa[2][4], fb[2][6];
    auto rd = [&](int set, lds_char* base, int s, int parts) {
      lds_char* b0 = base + boff0 + s * 4096; lds_char* b1 = base + boff1 + s * 4096;
      if (parts & 1) fb[set][0] = lds_tr2(b0, b0 + 512); else fb[set][2] = lds_tr2(b0 + 8192, b0 + 8192 + 512);
      fa[set][0] = lds_b128(base + (aoff ^ (s << 6)));
      fa[set][1] = lds_b128(base + (aoff ^ (s << 6)) + 1024);
      fa[set][2] = lds_b128(base + (aoff ^ (s << 6)) + 4096);
      fa[set][3] = lds_b128(base + (aoff ^ (s << 6)) + 5120);
      if (parts & 1) fb[set][1] = lds_tr2(b1, b1 + 512);
      if (parts == 3) fb[set][2] = lds_tr2(b0 + 8192, b0 + 8192 + 512);
      if (parts & 2) {
        fb[set][3] = lds_tr2(b1 + 8192, b1 + 8192 + 512);
        fb[set][4] = lds_tr2(b0 + 16384, b0 + 16384 + 512);
        fb[set][5] = lds_tr2(b1 + 16384, b1 + 16384 + 512);
      }
    };
    auto mm = [&](int set, int parts) {
#pragma unroll
      for (int n = (parts & 1) ? 0 : 2; n < ((parts & 2) ? 6 : 2); ++n)
#pragma unroll
        for (int m = 0; m < 4; ++m)
          acc[m][n] = n < 4 ? MFMA16(fb[set][n], fa[set][m], acc[m][n]) : MFMA16(fa[set][m], fb[set][n], acc[m][n]);
    };
    rd(0, smem, 0, 3);
    for (int kt = 0; kt < 12; ++kt) {
      lds_char* base = smem + (kt & 3) * STAGE;
      lds_char* nbase = smem + ((kt + 1) & 3) * STAGE;
      SB0();
      rd(1, base, 1, 3);
      mm(0, 3);
#pragma unroll
      for (int i = 0; i < 16; ++i) { SGB(0x8, 1); SGB(0x100, 1); }
      SGB(0x8, 8);
      SB0();
      WAIT_L0(); BAR();
      SB0();
      rd(0, nbase, 0, 3);
      mm(1, 3);
#pragma unroll
      for (int i = 0; i < 16; ++i) { SGB(0x8, 1); SGB(0x100, 1); }
      SGB(0x8, 8);
      SB0();
    }
    bf16x8 fx[8][4], fq[8][2];
    s16x4 fh[8][4];
    auto rdx = [&](int t) {
      lds_char* p = smem + (t >> 1) * STAGE + (aoff ^ ((t & 1) << 6));
      fx[t][0] = lds_b128(p); fx[t][1] = lds_b128(p + 1024); fx[t][2] = lds_b128(p + 4096); fx[t][3] = lds_b128(p + 5120);
    };
    auto rdw = [&](bf16x8* d, int t, int col, int which = 3) {
      lds_char* b0 = smem + (t >> 1) * STAGE + (t & 1) * 4096 + col * 8192;
      if (which & 1) d[0] = lds_tr2(b0 + boff0, b0 + boff0 + 512);
      if (which & 2) d[1] = lds_tr2(b0 + boff1, b0 + boff1 + 512);
    };
    asm volatile("" : "+v"(ep));
    const u64 w64 = (((u64)ep[1] << 32) | ep[0]) + 1;
    want = u32x2{(unsigned)w64, (unsigned)(w64 >> 32)};
    asm volatile("" : "+v"(want));
#pragma unroll
    for (int m = 0; m < 4; ++m) fx[0][m] = fa[0][m];
#pragma unroll
    for (int t = 0; t < 8; ++t) {
      SB0();
      if (t < 7) { rdw(&fb[(t + 1) & 1][2], t + 1, 1, 1); rdx(t + 1); rdw(&fb[(t + 1) & 1][2], t + 1, 1, 2); rdw(&fb[(t + 1) & 1][4], t + 1, 2); }
      else { rdw(fq[4], 4, 0); rdw(fq[5], 5, 0); rdw(fq[6], 6, 0); }
#pragma unroll
      for (int n = 2; n < 6; ++n)
#pragma unroll
        for (int m = 0; m < 4; ++m)
          acc[m][n] = n < 4 ? MFMA16(fb[t & 1][n], fx[t][m], acc[m][n]) : MFMA16(fx[t][m], fb[t & 1][n], acc[m][n]);
#pragma unroll
      for (int i = 0; i < 12; ++i) { SGB(0x8, 1); SGB(0x100, 1); }
      SGB(0x8, 4);
      SB0();
      if (t == 0 || t == 4) { FRESH(); BAR(); FRESH(); }
    }
    u32x4 kv[8];
    auto kv_cvt = [&](int u, int j) {
      const int mp = u >> 2, e = u & 1;
      const f32x4 a = (u & 2) ? acc[2 * mp + j][4 + e] : acc[2 * mp + e][2 + j];
      kv[u][2 * j] = pk2(a[0], a[1]); kv[u][2 * j + 1] = pk2(a[2], a[3]);
    };
    auto kv_out = [&](int u) {
      __builtin_amdgcn_raw_buffer_store_b128(kv[u], rkv, ((u & 2) ? vog : kog) + (u & 1) * ((u & 2) ? 256 : 128), kvo + (u >> 2) * 4096, 16);
    };
    auto kv_lds = [&](int u) { lds_st128(((u & 2) ? dv : dk) + (u >> 2) * 8192 + (u & 1) * ((u & 2) ? 256 : 128), kv[u]); };
    auto rd1 = [&](int t, int k) {
      lds_char* pw = smem + (t >> 1) * STAGE + (t & 1) * 4096 + (k & 2 ? boff1 : boff0) + (k & 1) * 512;
      lds_char* px = smem + (t >> 1) * STAGE + (aoff ^ ((t & 1) << 6)) + (k & 1) * 1024 + (k & 2) * 2048;
      if (k < 4) fh[t][k] = __builtin_amdgcn_ds_read_tr16_b64_v4i16((__attribute__((address_space(3))) s16x4*)pw);
      else fx[t][k - 4] = lds_b128(px);
    };
    auto m1 = [&](int t, int n, int m) {
      const u32x2 ua = __builtin_bit_cast(u32x2, fh[t][2 * n]), ub = __builtin_bit_cast(u32x2, fh[t][2 * n + 1]);
      const bf16x8 w = t >= 4 && t < 7 ? fq[t][n] : __builtin_bit_cast(bf16x8, u32x4{ua[0], ua[1], ub[0], ub[1]});
      acc[m][n] = MFMA16(w, fx[t][m], acc[m][n]);
    };
    auto q_out = [&](int m) {
      const f32x4 a = acc[m][0] * qs, c = acc[m][1] * qs;
      u32x4 v = {pk2(a[0], a[1]), pk2(a[2], a[3]), pk2(c[0], c[1]), pk2(c[2], c[3])};
      lds_st128((m >> 1 ? dq1 : dq0) + (m & 1) * 128, v);
    };
#pragma unroll
    for (int i = 0; i < 32; ++i) {
      SB0();
      m1(4 + (i >> 3), i >> 2 & 1, i & 3);
      SB0();
      if (i < 4) rd1(7, i); else if (i < 28) rd1((i - 4) >> 3, (i - 4) & 7); else rd1(3, i - 28);
      if (i % SPS < 2 && i / SPS < 8) kv_cvt(i / SPS, i % SPS);
      if (i >= 2 && (i - 2) % SPS == 0 && (i - 2) / SPS < 8) kv_out((i - 2) / SPS);
      if (i == 27) { FRESH(); BAR(); FRESH(); }
    }
    int ls = lane;
    asm volatile("" : "+v"(ls));
    SB0();
    setup(ls);
#pragma unroll
    for (int k = 4; k < 8; ++k) rd1(3, k);
#pragma unroll
    for (int u = 0; u < 8; ++u) kv_lds(u);
#pragma unroll
    for (int i = 0; i < 16; ++i) m1(i >> 3, i >> 2 & 1, i & 3);
#pragma unroll
    for (int i = 0; i < 4; ++i) { SGB(0x8, 1); SGB(0x100, 1); SGB(0x2, 4); }
#pragma unroll
    for (int i = 0; i < 4; ++i) { SGB(0x8, 1); SGB(0x200, 1); SGB(0x2, 3); SGB(0x8, 1); SGB(0x200, 1); SGB(0x2, 3); SGB(0x8, 1); SGB(0x2, 5); }
    pin();
#pragma unroll
    for (int m = 0; m < 4; ++m) {
      SB0();
      m1(2, 0, m); m1(2, 1, m); m1(3, 0, m); m1(3, 1, m);
      if (m) q_out(m - 1);
#pragma unroll
      for (int i = 0; i < 3; ++i) { SGB(0x8, 1); SGB(0x2, 3); }
      SGB(0x8, 1);
    }
    SB0();
    q_out(3);
    asm volatile("s_waitcnt vmcnt(0) lgkmcnt(0)" ::: "memory");
    __builtin_amdgcn_s_waitcnt(0x0070);
  }

  unsigned seq = ((unsigned)(KVPOS >> (32 + 4 * qb)) & 15) | ((unsigned)(KVPOS >> 32) << 4);
  int left = qb + 1;
  const int prb = wv < 4 ? rb - 2 + (wv >> 1) : -1;
  const bool fetch = prb >= 0;
  f32x16 negm;
  BAR();
  u32x2 kf_;
  const unsigned kfo = WS_FLK + (mek * 2 + (wv >> 1)) * 128;
  auto kpoll = [&]() { asm volatile("buffer_load_dwordx2 %0, off, %1, %2 sc1" : "=v"(kf_) : "s"(rsy), "s"(kfo) : "memory"); };
  if (wv == 4) __builtin_amdgcn_raw_buffer_store_b64(want, rsy, fk, 0, 16);
  if (fetch) kpoll();
  const bf16x8 q0 = lds_b128(qp), q1 = lds_b128(qp + 1024);
  lds_char *p0, *p1;
  bf16x8 kf[2], vf[2];
  f32x16 sa, sb;
  auto first = [&]() {
    p0 = kb + ((seq & 15) << 13);
    p1 = kb + ((seq >> 4 & 15) << 13);
    seq >>= 8;
    SB0();
    const bf16x8 k0 = lds_b128(p0), k1 = lds_b128(p0 + 1024);
    kf[0] = lds_b128(p1); kf[1] = lds_b128(p1 + 1024);
    vf[0] = lds_b128(p0 + 2048); vf[1] = lds_b128(p0 + 3072);
    SB0();
    sa = MFMA32(k1, q1, MFMA32(k0, q0, c));
  };
  first();
  {
    const float m = xhalf_max(max16(-INFINITY, sa));
#pragma unroll
    for (int i = 0; i < 16; ++i) { sa[i] -= m; negm[i] = -m; }
  }
  const unsigned kw0 = __builtin_amdgcn_readlane(want[0], 0), kw1 = __builtin_amdgcn_readlane(want[1], 0);
  auto kseen = [&]() {
    asm volatile("s_waitcnt vmcnt(0)" : "+v"(kf_) :: "memory");
    return __builtin_amdgcn_readfirstlane(kf_[0]) == kw0 && __builtin_amdgcn_readfirstlane(kf_[1]) == kw1;
  };
  if (fetch && !kseen()) kpoll();
#pragma unroll 1
  for (int phase = 0;; phase = 1) {
#pragma unroll 1
    for (;;) {
      p0 = kb + ((seq & 15) << 13);
      att_step(sa, sb, kf, vf, p1, p0, q0, q1, negm, ones, at);
      if (--left == 0) break;
      p1 = kb + ((seq >> 4 & 15) << 13);
      seq >>= 8;
      att_step(sb, sa, kf, vf, p0, p1, q0, q1, negm, ones, at);
      if (--left == 0) break;
    }
    if (phase) break;
    const int l2 = lane_id();
    {
      float nm = negm[0];
      asm volatile("" : "+v"(nm));
      const float ca = rb >= 1 ? nm : -INFINITY, cb = rb == 1 ? nm : -INFINITY;
      c = tri(l2, ca, cb);
      asm volatile("" : "+v"(c));
    }
    if (fetch) {
      const unsigned pos = (unsigned)(KVPOS >> (16 * (wv >> 1)));
      lds_char* d[4];
#pragma unroll
      for (int i = 0; i < 4; ++i) d[i] = smem + l2 * 16 + (ha * 4096 + ((pos >> 4 * i & 15) << 13));
      asm volatile("" : "+v"(d[0]), "+v"(d[1]), "+v"(d[2]), "+v"(d[3]));
#pragma unroll 1
      for (unsigned spins = 0; spins < SPIN_CAP; ++spins) {
        if (kseen()) break;
        kpoll();
      }
      asm volatile("s_waitcnt vmcnt(0)" : "+v"(kf_) :: "memory");
      fetch16(wa, rkv, l2 * 16, (hp * 16 + prb) * 32768 + ha * 16384, d[0], d[1], d[2], d[3]);
    }
    if (PRI && wv >= 4) __builtin_amdgcn_s_setprio(1);
    const int j0 = rb >= 2 ? qb : 8 - 4 * rb;
    seq = (unsigned)(KVPOS >> (4 * j0));
    left = rb ? 8 - j0 : 1;
    asm volatile("" : "+s"(seq));
    WAIT_L0();
    BAR();
    first();
  }
  const int l3 = lane_id(), r3 = l3 & 31, h3 = l3 >> 5;
  {
    f32x2 z0 = {0.f, 0.f}, z1 = z0;
#pragma unroll
    for (int i = 0; i < 16; i += 4) {
      z0 = __builtin_elementwise_fma(f32x2{at.o[i], at.o[i + 1]}, f32x2{0.f, 0.f}, z0);
      z1 = __builtin_elementwise_fma(f32x2{at.o[i + 2], at.o[i + 3]}, f32x2{0.f, 0.f}, z1);
    }
    z0 += z1;
    const float z = z0[0] + z0[1];
    if (__builtin_expect(__any(!(at.l[0] < 1e30f) || z != 0.f), 0))
      attn_exact(kb, q0, q1, r3, h3, qb, rb >= 2 ? qb : 8 - rb * 4, ones, at);
    const float inv = __builtin_amdgcn_rcpf(at.l[0]);
#pragma unroll
    for (int i = 0; i < 16; ++i) at.o[i] *= inv;
    const unsigned vo = (r3 >> 4) * 1024 + h3 * 256 + (r3 & 15) * 16;
    const unsigned so = (unsigned)(((rb * 4 + qb) * 32 + hp * 2 + ha) * 2048);
    __builtin_amdgcn_raw_buffer_store_b128(__builtin_bit_cast(u32x4, pack8(at.o, 0)), rof, vo, so, 16);
    __builtin_amdgcn_raw_buffer_store_b128(__builtin_bit_cast(u32x4, pack8(at.o, 1)), rof, vo + 512, so, 16);
    if (wv >= 4) {
      if (PRI) __builtin_amdgcn_s_setprio(0);
#pragma unroll
      for (int u = 0; u < 16; ++u)
        lds_st128(smem + ((u >> 2) * 4 + lw < 6 ? L_WA0 : L_WA1) + ((u >> 2) * 4 + lw) * 4096 + (u & 3) * 1024 + l3 * 16, wa[u]);
    }
    WAIT_V(0);
  }
  BAR();

  const int qo = wv & 3, kh = wv >> 2;
  if (kh) {
    if (wv == 4) __builtin_amdgcn_raw_buffer_store_b64(want, rsy, l3 < 16 ? WS_FLO + ((rb * 16 + l3) * 16 + hp) * 128 : OOB, 0, 16);
#pragma unroll
    for (int u = 0; u < 16; ++u) lds_st128(smem + L_WB + ((u >> 2) * 4 + qo) * 4096 + (u & 3) * 1024 + l3 * 16, hold[u]);
    WAIT_L0();
  } else if (wv == 0) {
    const unsigned w0 = __builtin_amdgcn_readlane(want[0], 1), w1 = __builtin_amdgcn_readlane(want[1], 1);
#pragma unroll 1
    for (unsigned spins = 0; spins < SPIN_CAP; ++spins) {
      FRESH();
      u32x2 f = __builtin_amdgcn_raw_buffer_load_b64(rsy, (l3 & 15) * 128, WS_FLO + me * 2048, 16);
      if (__all(f[0] == w0 && f[1] == w1)) break;
    }
  }
  BAR();
  u32x4 of[32];
  const unsigned ob = (unsigned)(((rbo * 4 + qo) * 32 + kh * 16) * 2048), ov0 = l3 * 16 + kh * 1024, ov1 = ov0 ^ 1024;
  auto o_in = [&](int i) {
    of[2 * i] = __builtin_amdgcn_raw_buffer_load_b128(rof, ov0 + (i & 1) * 2048, ob + (i >> 1) * 4096, 16);
    of[2 * i + 1] = __builtin_amdgcn_raw_buffer_load_b128(rof, ov1 + (i & 1) * 2048, ob + (i >> 1) * 4096, 16);
  };
#pragma unroll
  for (int i = 0; i < OD; ++i) o_in(i);
  f32x4 oc[2][4];
#pragma unroll
  for (int i = 0; i < 8; ++i) oc[i >> 2][i & 3] = f32x4{0, 0, 0, 0};
  {
    const int kg = l3 >> 4, q4 = (l3 & 15) >> 2, p4 = l3 & 3;
    const int swz = 2 * (kg & 1) + (q4 >> 1);
    const unsigned rowo = (16 * (kg >> 1) + 4 * (kg & 1) + q4) * 128 + p4 * 8;
    lds_char* wb0 = smem + (kh ? L_WB : L_WA0) + rowo;
    lds_char* wb1 = smem + (kh ? L_WB : L_WA1) + rowo;
    bf16x8 wf[2][4];
    auto rdw = [&](int set, int i) {
      lds_char* base = (i < 6 ? wb0 : wb1) + i * 4096;
#pragma unroll
      for (int c = 0; c < 4; ++c) {
        lds_char* p = base + ((c ^ swz) << 5);
        wf[set][c] = lds_tr2(p, p + 1024);
      }
    };
    rdw(0, 0);
#pragma unroll
    for (int i = 0; i < 16; ++i) {
      SB0();
      if (i + OD < 16) o_in(i + OD);
      SB0();
      if (i < 15) rdw((i + 1) & 1, i + 1);
#pragma unroll
      for (int c = 0; c < 4; ++c)
#pragma unroll
        for (int q = 0; q < 2; ++q) oc[q][c] = MFMA16(wf[i & 1][c], __builtin_bit_cast(bf16x8, of[2 * i + q]), oc[q][c]);
#pragma unroll
      for (int k = 0; k < 8; ++k) { SGB(0x8, 1); SGB(0x100, 1); }
      SB0();
    }
  }
  {
    lds_char* xp = smem + (kh ? L_XC1 : L_XC0) + qo * 4096 + l3 * 16;
    lds_char* xq = smem + (kh ? L_XC0 : L_XC1) + qo * 4096 + l3 * 16;
#pragma unroll
    for (int c = 0; c < 4; ++c) lds_st128(xp + c * 1024, __builtin_bit_cast(u32x4, oc[1][c]));
    WAIT_L0();
    BAR();
    f32x4 fin[4];
#pragma unroll
    for (int c = 0; c < 4; ++c) fin[c] = oc[0][c] + *(__attribute__((address_space(3))) f32x4*)(xq + c * 1024);
    const int g4 = l3 >> 4;
    __attribute__((address_space(1))) char* dst = (__attribute__((address_space(1))) char*)out + (size_t)(rbo * 128 + qo * 32 + kh * 16 + (l3 & 15)) * 2048 + cbo * 128 + (g4 & 1) * 32 + (g4 >> 1) * 16;
#pragma unroll
    for (int u = 0; u < 2; ++u) {
      unsigned a0 = pk2(fin[2 * u][0], fin[2 * u][1]), a1 = pk2(fin[2 * u][2], fin[2 * u][3]);
      unsigned b0 = pk2(fin[2 * u + 1][0], fin[2 * u + 1][1]), b1 = pk2(fin[2 * u + 1][2], fin[2 * u + 1][3]);
      auto s0 = __builtin_amdgcn_permlane16_swap(a0, b0, false, false);
      auto s1 = __builtin_amdgcn_permlane16_swap(a1, b1, false, false);
      u32x4 v = {s0[0], s1[0], s0[1], s1[1]};
      *(__attribute__((address_space(1))) u32x4*)(dst + u * 64) = v;
    }
    if (wv == 0) __builtin_amdgcn_raw_buffer_store_b64(want, rsy, l3 == 0 ? WS_EPK + mek * 8 : l3 == 1 ? WS_EPO + me * 8 : OOB, 0, 16);
  }
  asm volatile(".Lsw_end:");
}

extern "C" void kernel_launch(void* const* d_in, const int* in_sizes, int n_in, void* d_out, int out_size,
                              void* d_ws, size_t ws_size, hipStream_t stream) {
  (void)hipFuncSetAttribute((const void*)k_fused, hipFuncAttributeMaxDynamicSharedMemorySize, LDS_BYTES);
  k_fused<<<256, 512, LDS_BYTES, stream>>>((const __hip_bfloat16*)d_in[0], (const __hip_bfloat16*)d_in[1],
                                           (const __hip_bfloat16*)d_in[2], (__hip_bfloat16*)d_out, (char*)d_ws);
}
```
